```python
import torch

def _softmax(x, axis=-1):
    return torch.softmax(x, dim=axis)

def _gelu(x):
    return torch.nn.functional.gelu(x, approximate='tanh')

def _silu(x):
    return torch.nn.functional.silu(x)

def _sigmoid(x):
    return torch.sigmoid(x)

def _layernorm(x, axis=-1, eps=1e-05):
    m = x.mean(dim=axis, keepdim=True)
    v = x.var(dim=axis, keepdim=True, unbiased=False)
    return (x - m) / torch.sqrt(v + eps)

def _scatter_add(tgt, idx, src, axis=0):
    return tgt.clone().index_add_(axis, idx, src)

def _segment_sum(src, seg, n):
    out = torch.zeros((n,) + src.shape[1:], dtype=src.dtype)
    return out.index_add_(0, seg, src)

def _pad(x, pw):
    flat = tuple((v for pair in reversed(pw) for v in pair))
    return torch.nn.functional.pad(x, flat)
kernel_name = 'adaLN_cond__mha_block_cached__swiglu_ffn__decode'

def setup_inputs():

    _w = lambda a: (lambda t: t.to(torch.bfloat16) if t.is_floating_point() else t)(torch.as_tensor(a))

    def _A_setup():
        B, Sq, D = (8, 1, 512)
        H, Sk, Dh = (32, 4096, 128)
        return {'x': _w((lambda *s: torch.randn(*s, dtype=torch.bfloat16) * 0.1)(B, Sq, D)), 'k_cache': _w((lambda *s: torch.randn(*s, dtype=torch.bfloat16) * 0.8)(B, H, Sk, Dh)), 'v_cache': _w((lambda *s: torch.randn(*s, dtype=torch.bfloat16) * 1.6)(B, H, Sk, Dh)), 'wq': _w((lambda *s: torch.randn(*s, dtype=torch.bfloat16) * 0.8)(D, H * Dh) * D ** (-0.5)), 'wo': _w((lambda *s: torch.randn(*s, dtype=torch.bfloat16) * 1.6)(H * Dh, D) * (H * Dh) ** (-0.5))}
    _A_in = _A_setup()

    def _B_setup():
        B, S, D, F = (8, 1, 512, 1376)
        return {'x': _w((lambda *s: torch.randn(*s, dtype=torch.bfloat16) * 0.1)(B, S, D)), 'w_gate': _w((lambda *s: torch.randn(*s, dtype=torch.bfloat16) * 0.1)(D, F)), 'w_up': _w((lambda *s: torch.randn(*s, dtype=torch.bfloat16) * 0.1)(D, F)), 'w_down': _w((lambda *s: torch.randn(*s, dtype=torch.bfloat16) * 0.1)(F, D))}
    _B_in = _B_setup()
    return {'A_x': _A_in['x'], 'A_k_cache': _A_in['k_cache'], 'A_v_cache': _A_in['v_cache'], 'A_wq': _A_in['wq'], 'A_wo': _A_in['wo'], 'B_x': _B_in['x'], 'B_w_gate': _B_in['w_gate'], 'B_w_up': _B_in['w_up'], 'B_w_down': _B_in['w_down'], 'c': _w((lambda *s: torch.randn(*s, dtype=torch.bfloat16) * 0.8)(8, 512) * 8.0), 'w_mod': _w((lambda *s: torch.randn(*s, dtype=torch.bfloat16) * 0.8)(512, 6 * 512) * 512 ** (-0.5) * 8.0)}

def reference(A_x, A_k_cache, A_v_cache, A_wq, A_wo, B_x, B_w_gate, B_w_up, B_w_down, c, w_mod):

    def _A_ref(x, k_cache, v_cache, wq, wo):
        B, Sq, D = x.shape
        H, Dh = (32, 128)
        q = (lambda x, *d: x.permute(*d))((x @ wq).reshape(B, Sq, H, Dh), 0, 2, 1, 3)
        scores = torch.einsum('bhqd,bhkd->bhqk', q, k_cache) * Dh ** (-0.5)
        p = _softmax(scores, axis=-1)
        o = torch.einsum('bhqk,bhkd->bhqd', p, v_cache)
        return (lambda x, *d: x.permute(*d))(o, 0, 2, 1, 3).reshape(B, Sq, H * Dh) @ wo

    def _B_ref(x, w_gate, w_up, w_down):
        g = _silu(x @ w_gate)
        u = x @ w_up
        return g * u @ w_down
    x = A_x
    mod = (_silu(c) @ w_mod).reshape(c.shape[0], 6, 1, -1)
    sa, ca, ga, sb, cb, gb = (mod[:, 0].clone(), mod[:, 1].clone(), mod[:, 2].clone(), mod[:, 3].clone(), mod[:, 4].clone(), mod[:, 5].clone())
    h = _layernorm(x, axis=-1) * (1 + ca) + sa
    x = x + ga * _A_ref(h, A_k_cache, A_v_cache, A_wq, A_wo)
    h = _layernorm(x, axis=-1) * (1 + cb) + sb
    x = x + gb * _B_ref(h, B_w_gate, B_w_up, B_w_down)
    return x
```

```cpp
#include <hip/hip_runtime.h>
#include <hip/hip_bf16.h>
#include <stdint.h>

typedef unsigned int u32;
typedef unsigned long long u64;
using u32x4 = __attribute__((ext_vector_type(4))) unsigned;
using u32x2 = __attribute__((ext_vector_type(2))) unsigned;
using f32x4 = __attribute__((ext_vector_type(4))) float;
using f32x2 = __attribute__((ext_vector_type(2))) float;
using bf16x2 = __attribute__((ext_vector_type(2))) __bf16;
typedef __attribute__((address_space(1))) unsigned long long gu64;
typedef __attribute__((address_space(1))) unsigned gu32;
#define RLX_AGENT __ATOMIC_RELAXED, __HIP_MEMORY_SCOPE_AGENT

constexpr int NB = 8, DM = 512, NH = 32, SK = 4096, DH = 128, FF = 1376;
constexpr int NT = 512, NWV = 8;
constexpr int NFW = 22;
constexpr int FIN0 = 24;
constexpr int TL = 4;
constexpr int NTILE = SK / (4 * TL);
constexpr u32 SENT = 0xFFFFFFFFu;
constexpr u64 TAGMASK = 0xFFFFFFFFFFFFull;
constexpr unsigned SPIN_CAP = 1u << 20;

constexpr size_t WS_EP = 0;
constexpr size_t WS_G1 = 4096;
constexpr size_t WS_G2 = WS_G1 + 8 * 8192;
constexpr size_t WS_PA = WS_G2 + 8 * 16384;
constexpr size_t WS_PB = WS_PA + 8 * 32 * 2048;
constexpr size_t WS_END = WS_PB + 8 * 32 * 2048;
constexpr size_t WS_STAMP = WS_END;

constexpr int L_WO = 0;
constexpr int L_RED = 131072;
constexpr int L_SC = L_RED + 16384;
constexpr int L_HP = L_SC + 2048;
constexpr int L_Q = L_HP + 1024;
constexpr int L_MRG = L_Q + 256;
constexpr int L_ST = L_MRG + 8 * 132 * 4;
constexpr int L_CNT = L_ST + 128;
constexpr int L_RED2 = L_CNT + 16;
constexpr int L_STAMP = L_RED2 + 2048;
constexpr int LDS_BYTES = L_STAMP + 256;

struct Params {
  const unsigned short* x; const unsigned short* kc; const unsigned short* vc; const unsigned short* wq; const unsigned short* wo;
  const unsigned short* wg; const unsigned short* wu; const unsigned short* wd; const unsigned short* c; const unsigned short* wmod;
  unsigned short* out; unsigned char* ws;
};

__device__ __forceinline__ float bf2f(unsigned short v) { return __builtin_bit_cast(float, (u32)v << 16); }
__device__ __forceinline__ unsigned short f2bf(float f) { return __builtin_bit_cast(unsigned short, (__bf16)f); }
__device__ __forceinline__ float rb(float f) { return (float)(__bf16)f; }
__device__ __forceinline__ float lo16(u32 x) { return __builtin_bit_cast(float, x << 16); }
__device__ __forceinline__ float hi16(u32 x) { return __builtin_bit_cast(float, x & 0xffff0000u); }
__device__ __forceinline__ float dot2(u32 a, u32 b, float acc) {
  return __builtin_amdgcn_fdot2_f32_bf16(__builtin_bit_cast(bf16x2, a), __builtin_bit_cast(bf16x2, b), acc, false);
}
template <int CTRL> __device__ __forceinline__ float dpp(float x) {
  return __builtin_bit_cast(float, __builtin_amdgcn_mov_dpp(__builtin_bit_cast(int, x), CTRL, 0xf, 0xf, true));
}
constexpr int ROR1 = 0x121, ROR2 = 0x122, ROR4 = 0x124, ROR8 = 0x128;
__device__ __forceinline__ float row16_sum(float x) { x += dpp<ROR8>(x); x += dpp<ROR4>(x); x += dpp<ROR2>(x); x += dpp<ROR1>(x); return x; }
__device__ __forceinline__ float tr16(float a, float b) {
  auto s = __builtin_amdgcn_permlane16_swap(__builtin_bit_cast(u32, a), __builtin_bit_cast(u32, b), false, false);
  return __uint_as_float(s[0]) + __uint_as_float(s[1]);
}
__device__ __forceinline__ float tr32(float a, float b) {
  auto s = __builtin_amdgcn_permlane32_swap(__builtin_bit_cast(u32, a), __builtin_bit_cast(u32, b), false, false);
  return __uint_as_float(s[0]) + __uint_as_float(s[1]);
}
__device__ __forceinline__ float x16_sum(float x) { return tr16(x, x); }
__device__ __forceinline__ float x32_sum(float x) { return tr32(x, x); }
__device__ __forceinline__ float x16_max(float x) {
  auto s = __builtin_amdgcn_permlane16_swap(__builtin_bit_cast(u32, x), __builtin_bit_cast(u32, x), false, false);
  return fmaxf(__uint_as_float(s[0]), __uint_as_float(s[1]));
}
__device__ __forceinline__ float x32_max(float x) {
  auto s = __builtin_amdgcn_permlane32_swap(__builtin_bit_cast(u32, x), __builtin_bit_cast(u32, x), false, false);
  return fmaxf(__uint_as_float(s[0]), __uint_as_float(s[1]));
}
__device__ __forceinline__ float wave_sum(float x) { return x32_sum(x16_sum(row16_sum(x))); }
__device__ __forceinline__ u32 pk_lo(u32 a, u32 b) { return __builtin_amdgcn_perm(b, a, 0x05040100u); }
__device__ __forceinline__ u32 pk_hi(u32 a, u32 b) { return __builtin_amdgcn_perm(b, a, 0x07060302u); }
__device__ __forceinline__ u32 clean(float v) { u32 u = __builtin_bit_cast(u32, v); return u == SENT ? 0x7FC00000u : u; }

#define BAR() do { asm volatile("s_waitcnt lgkmcnt(0)" ::: "memory"); __builtin_amdgcn_s_barrier(); asm volatile("" ::: "memory"); } while (0)
#define LDSWAIT() asm volatile("s_waitcnt lgkmcnt(0)" ::: "memory")
#define CFENCE() asm volatile("" ::: "memory")
#define SB() do { asm volatile("" ::: "memory"); __builtin_amdgcn_sched_barrier(0); } while (0)
#define RSRC(p, n) __builtin_amdgcn_make_buffer_rsrc((void*)(p), 0, (n), 0x00020000)
#define BLD(r, vo, so, aux) __builtin_amdgcn_raw_buffer_load_b128((r), (vo), (so), (aux))

#define STAMP(i) do {} while (0)
#define STAMPW(i, ww) do {} while (0)
#define STAMPFLUSH() do {} while (0)

__device__ __forceinline__ float ln_norm(float val, float* st, int lane, int w) {
  const float sw = wave_sum(val);
  const float mw = sw * (1.0f / 64.0f);
  const float dv = val - mw;
  const float m2 = wave_sum(dv * dv);
  if (lane == 0) { st[2 * w] = sw; st[2 * w + 1] = m2; }
  BAR();
  const f32x4 a = *(const f32x4*)(st), b = *(const f32x4*)(st + 4), c = *(const f32x4*)(st + 8), d = *(const f32x4*)(st + 12);
  const float tot = ((a[0] + a[2]) + (b[0] + b[2])) + ((c[0] + c[2]) + (d[0] + d[2]));
  const float mean = tot * (1.0f / 512.0f);
  float M2 = ((a[1] + a[3]) + (b[1] + b[3])) + ((c[1] + c[3]) + (d[1] + d[3]));
  float e;
  e = a[0] * (1.0f / 64.0f) - mean; float q2 = e * e;
  e = a[2] * (1.0f / 64.0f) - mean; q2 += e * e;
  e = b[0] * (1.0f / 64.0f) - mean; q2 += e * e;
  e = b[2] * (1.0f / 64.0f) - mean; q2 += e * e;
  e = c[0] * (1.0f / 64.0f) - mean; q2 += e * e;
  e = c[2] * (1.0f / 64.0f) - mean; q2 += e * e;
  e = d[0] * (1.0f / 64.0f) - mean; q2 += e * e;
  e = d[2] * (1.0f / 64.0f) - mean; q2 += e * e;
  M2 += 64.0f * q2;
  const float var = rb(M2 * (1.0f / 512.0f));
  const float den = rb(sqrtf(rb(var + 1e-5f)));
  return rb(rb(val - rb(mean)) / den);
}

__global__ void __launch_bounds__(NT, 2) fused_block_kernel(Params p) {
  extern __shared__ __attribute__((aligned(16))) unsigned char smem[];
  const int tid = threadIdx.x, lane = tid & 63;
  const int w = __builtin_amdgcn_readfirstlane(tid >> 6);
  const int b = blockIdx.x >> 5, h = blockIdx.x & 31;
  STAMP(0);

  float* const sRed = (float*)(smem + L_RED);
  float* const sSc = (float*)(smem + L_SC);
  float* const sMrg = (float*)(smem + L_MRG);
  float* const sSt = (float*)(smem + L_ST);
  float* const sRed2 = (float*)(smem + L_RED2);

  const __amdgpu_buffer_rsrc_t rPA = RSRC(p.ws + WS_PA + (size_t)b * 32 * 2048, 32 * 2048);
  const __amdgpu_buffer_rsrc_t rPB = RSRC(p.ws + WS_PB + (size_t)b * 32 * 2048, 32 * 2048);
  if (lane < 32) {
    const u32x4 sv = {SENT, SENT, SENT, SENT};
    const int o = h * 2048 + (w & 3) * 512 + lane * 16;
    if (w < 4) __builtin_amdgcn_raw_buffer_store_b128(sv, rPA, o, 0, 16);
    else __builtin_amdgcn_raw_buffer_store_b128(sv, rPB, o, 0, 16);
  }
  CFENCE();
  u64 ep = __hip_atomic_load((gu64*)(p.ws + WS_EP + b * 256), RLX_AGENT);
  const unsigned short cv = p.c[b * DM + tid];
  const unsigned short xv = p.x[b * DM + tid];
  SB();
  STAMP(16);
  const __amdgpu_buffer_rsrc_t rWM = RSRC(p.wmod, DM * 6 * DM * 2);
  u32x4 wm1[4];
#pragma unroll
  for (int i = 0; i < 4; ++i) wm1[i] = BLD(rWM, (w * 64 + i * 16 + (lane >> 2)) * 6144 + h * 64 + (lane & 3) * 16, 0, 0);
  SB();
  STAMP(17);
  const __amdgpu_buffer_rsrc_t rWQ = RSRC(p.wq, DM * NH * DH * 2);
  u32x4 wqr[16];
#pragma unroll
  for (int i = 0; i < 16; ++i) wqr[i] = BLD(rWQ, (w * 64 + i * 4 + (lane >> 4)) * 8192 + h * 256 + (lane & 15) * 16, 0, 0);
  SB();
  STAMP(18);
  u32x4 wm2[8];
#pragma unroll
  for (int i = 0; i < 8; ++i) wm2[i] = BLD(rWM, (w * 64 + i * 8 + (lane >> 3)) * 6144 + 2048 + h * 128 + (lane & 7) * 16, 0, 0);
  SB();
  STAMP(19);
  const __amdgpu_buffer_rsrc_t rK = RSRC(p.kc + (size_t)blockIdx.x * SK * DH, SK * DH * 2);
  const __amdgpu_buffer_rsrc_t rV = RSRC(p.vc + (size_t)blockIdx.x * SK * DH, SK * DH * 2);
  const int vo = lane * 16;
  u32x4 KA[TL], VA[TL], KB[TL], VB[TL];
#define LOADT(dst, r, t) do { _Pragma("unroll") for (int i_ = 0; i_ < TL; ++i_) dst[i_] = BLD(r, vo + i_ * 1024, (t) * (TL * 1024), 2); } while (0)
  LOADT(KA, rK, w);
  SB();
  STAMP(20);
  LOADT(VA, rV, w);
  SB();
  STAMP(21);
  LOADT(KB, rK, NWV + w); LOADT(VB, rV, NWV + w);
  SB();

  STAMP(12);
  float cf = bf2f(cv);
  sSc[tid] = rb(cf / (1.0f + __expf(-cf)));
  const float xf = bf2f(xv);
  const float y1 = ln_norm(xf, sSt, lane, w);
  STAMP(1);
  asm volatile("" : "+v"(ep));
  const u64 tag = (ep + 1) & TAGMASK;
  {
    float a[8] = {0, 0, 0, 0, 0, 0, 0, 0};
#pragma unroll
    for (int i = 0; i < 4; ++i) {
      const float s = sSc[w * 64 + i * 16 + (lane >> 2)];
#pragma unroll
      for (int j = 0; j < 4; ++j) { a[2 * j] = fmaf(s, lo16(wm1[i][j]), a[2 * j]); a[2 * j + 1] = fmaf(s, hi16(wm1[i][j]), a[2 * j + 1]); }
    }
#pragma unroll
    for (int j = 0; j < 8; ++j) { a[j] += dpp<ROR4>(a[j]); a[j] += dpp<ROR8>(a[j]); }
    float r4[4], r2[2];
#pragma unroll
    for (int j = 0; j < 4; ++j) r4[j] = tr16(a[j], a[j + 4]);
#pragma unroll
    for (int j = 0; j < 2; ++j) r2[j] = tr32(r4[j], r4[j + 2]);
    if ((lane & 12) == 0) {
      const int col = (lane & 3) * 8 + ((lane >> 4) & 1) * 4 + (lane >> 5) * 2;
      *(f32x2*)(sRed + w * 32 + col) = f32x2{r2[0], r2[1]};
    }
    BAR();
    if (tid < 32) {
      float t = 0.f;
#pragma unroll
      for (int i = 0; i < 8; ++i) t += sRed[i * 32 + tid];
      const int col = h * 32 + tid;
      __hip_atomic_store((gu64*)(p.ws + WS_G1 + b * 8192) + (col & 511) * 2 + (col >> 9), (tag << 16) | f2bf(t), RLX_AGENT);
    }
  }
  STAMP(2);
  {
    float a[8] = {0, 0, 0, 0, 0, 0, 0, 0};
#pragma unroll
    for (int i = 0; i < 8; ++i) {
      const float s = sSc[w * 64 + i * 8 + (lane >> 3)];
#pragma unroll
      for (int j = 0; j < 4; ++j) { a[2 * j] = fmaf(s, lo16(wm2[i][j]), a[2 * j]); a[2 * j + 1] = fmaf(s, hi16(wm2[i][j]), a[2 * j + 1]); }
    }
#pragma unroll
    for (int j = 0; j < 8; ++j) a[j] += dpp<ROR8>(a[j]);
    float r4[4], r2[2];
#pragma unroll
    for (int j = 0; j < 4; ++j) r4[j] = tr16(a[j], a[j + 4]);
#pragma unroll
    for (int j = 0; j < 2; ++j) r2[j] = tr32(r4[j], r4[j + 2]);
    if ((lane & 8) == 0) {
      const int col = (lane & 7) * 8 + ((lane >> 4) & 1) * 4 + (lane >> 5) * 2;
      *(f32x2*)(sRed2 + w * 64 + col) = f32x2{r2[0], r2[1]};
    }
  }
  u32 wqp[8][8];
#pragma unroll
  for (int j = 0; j < 8; ++j)
#pragma unroll
    for (int e = 0; e < 4; ++e) { wqp[j][2 * e] = pk_lo(wqr[2 * j][e], wqr[2 * j + 1][e]); wqp[j][2 * e + 1] = pk_hi(wqr[2 * j][e], wqr[2 * j + 1][e]); }
  BAR();
  if (tid >= 64 && tid < 128) {
    const int t64 = tid - 64;
    float t = 0.f;
#pragma unroll
    for (int i = 0; i < 8; ++i) t += sRed2[i * 64 + t64];
    __hip_atomic_store((gu64*)(p.ws + WS_G2 + b * 16384) + h * 64 + t64, (tag << 16) | f2bf(t), RLX_AGENT);
  }
  STAMP(3);
  float hval;
  {
    const __amdgpu_buffer_rsrc_t rG1 = RSRC(p.ws + WS_G1 + b * 8192, 8192);
    const u32 tlo = (u32)(tag << 16) >> 16, thi = (u32)(tag >> 16);
    u32x4 g;
    for (unsigned spin = 0;; ++spin) {
      g = BLD(rG1, tid * 16, 0, 16);
      const bool ok = (g[0] >> 16) == tlo && g[1] == thi && (g[2] >> 16) == tlo && g[3] == thi;
      if (__all(ok) || spin > SPIN_CAP) break;
      __builtin_amdgcn_s_sleep(2);
    }
    const float sa = lo16(g[0]), ca = lo16(g[2]);
    hval = rb(rb(y1 * rb(1.0f + ca)) + sa);
  }
  STAMP(4);
  {
    const int r = lane;
    ((unsigned short*)(smem + L_HP))[((w * 4 + (r & 3)) * 8 + (r >> 3)) * 2 + ((r >> 2) & 1)] = f2bf(hval);
    LDSWAIT();
    const u32x4 h0 = *(const u32x4*)(smem + L_HP + (w * 4 + (lane >> 4)) * 32), h1 = *(const u32x4*)(smem + L_HP + (w * 4 + (lane >> 4)) * 32 + 16);
    float a[8] = {0, 0, 0, 0, 0, 0, 0, 0};
#pragma unroll
    for (int j = 0; j < 8; ++j) {
      const u32 hp = j < 4 ? h0[j & 3] : h1[j & 3];
#pragma unroll
      for (int e = 0; e < 8; ++e) a[e] = dot2(wqp[j][e], hp, a[e]);
    }
    float r4[4], r2[2];
#pragma unroll
    for (int j = 0; j < 4; ++j) r4[j] = tr16(a[j], a[j + 4]);
#pragma unroll
    for (int j = 0; j < 2; ++j) r2[j] = tr32(r4[j], r4[j + 2]);
    const int col = (lane & 15) * 8 + ((lane >> 4) & 1) * 4 + (lane >> 5) * 2;
    *(f32x2*)(sRed + w * 128 + col) = f32x2{r2[0], r2[1]};
    BAR();
    if (tid < 128) {
      float t = 0.f;
#pragma unroll
      for (int i = 0; i < 8; ++i) t += sRed[i * 128 + tid];
      ((unsigned short*)(smem + L_Q))[tid] = f2bf(t);
    }
    if (tid == 128 && h == 0) __hip_atomic_store((gu64*)(p.ws + WS_EP + b * 256), tag, RLX_AGENT);
    BAR();
  }
  const u32x4 qv = *(const u32x4*)(smem + L_Q + (lane & 15) * 16);
  STAMP(5);

  float m = -INFINITY, l = 0.f;
  f32x2 acc[4] = {{0, 0}, {0, 0}, {0, 0}, {0, 0}};
  int tnext = 2 * NWV + w;
  constexpr float SCALE = 0.08838834764831845f, LOG2E = 1.4426950408889634f;
#define STEP(KX, VX) do { \
    float s_[TL]; \
    _Pragma("unroll") for (int i_ = 0; i_ < TL; ++i_) { float d_ = dot2(KX[i_][0], qv[0], 0.f); d_ = dot2(KX[i_][1], qv[1], d_); d_ = dot2(KX[i_][2], qv[2], d_); s_[i_] = dot2(KX[i_][3], qv[3], d_); } \
    asm volatile("" : "+v"(s_[0]), "+v"(s_[1]), "+v"(s_[2]), "+v"(s_[3])); \
    SB(); \
    LOADT(KX, rK, tnext); \
    SB(); \
    _Pragma("unroll") for (int i_ = 0; i_ < TL; ++i_) s_[i_] = rb(rb(row16_sum(s_[i_])) * SCALE); \
    const float mn_ = fmaxf(fmaxf(fmaxf(s_[0], s_[1]), fmaxf(s_[2], s_[3])), m); \
    const float al_ = __builtin_amdgcn_exp2f((m - mn_) * LOG2E); \
    m = mn_; \
    float p_[TL]; \
    _Pragma("unroll") for (int i_ = 0; i_ < TL; ++i_) p_[i_] = __builtin_amdgcn_exp2f((s_[i_] - mn_) * LOG2E); \
    l = fmaf(l, al_, (p_[0] + p_[1]) + (p_[2] + p_[3])); \
    _Pragma("unroll") for (int j_ = 0; j_ < 4; ++j_) acc[j_] *= al_; \
    _Pragma("unroll") for (int i_ = 0; i_ < TL; ++i_) { const f32x2 pp_ = {p_[i_], p_[i_]}; \
      _Pragma("unroll") for (int j_ = 0; j_ < 4; ++j_) acc[j_] = __builtin_elementwise_fma(f32x2{lo16(VX[i_][j_]), hi16(VX[i_][j_])}, pp_, acc[j_]); } \
    asm volatile("" : "+v"(acc[0]), "+v"(acc[1]), "+v"(acc[2]), "+v"(acc[3]), "+v"(m), "+v"(l)); \
    SB(); \
    LOADT(VX, rV, tnext); \
    SB(); \
    tnext += NWV; \
  } while (0)
#define STEP2() do { STEP(KA, VA); STEP(KB, VB); } while (0)

  STEP2();
  SB();
  const __amdgpu_buffer_rsrc_t rWO = RSRC(p.wo, NH * DH * DM * 2);
  u32x4 wor[16];
#pragma unroll
  for (int i = 0; i < 16; ++i) wor[i] = BLD(rWO, (h * 128 + w * 16 + i) * 1024 + lane * 16, 0, 0);
  SB();
  STEP2();
  STEP2();
  SB();
#pragma unroll
  for (int j = 0; j < 8; ++j) {
    u32x4 a, c2;
#pragma unroll
    for (int e = 0; e < 2; ++e) { a[2 * e] = pk_lo(wor[2 * j][e], wor[2 * j + 1][e]); a[2 * e + 1] = pk_hi(wor[2 * j][e], wor[2 * j + 1][e]);
                                  c2[2 * e] = pk_lo(wor[2 * j][e + 2], wor[2 * j + 1][e + 2]); c2[2 * e + 1] = pk_hi(wor[2 * j][e + 2], wor[2 * j + 1][e + 2]); }
    *(u32x4*)(smem + L_WO + ((w * 8 + j) * 2) * 1024 + lane * 16) = a;
    *(u32x4*)(smem + L_WO + ((w * 8 + j) * 2 + 1) * 1024 + lane * 16) = c2;
  }
  SB();
  const bool ffn = h < NFW;
  const __amdgpu_buffer_rsrc_t rWG = RSRC(p.wg, ffn ? DM * FF * 2 : 0);
  const __amdgpu_buffer_rsrc_t rWU = RSRC(p.wu, ffn ? DM * FF * 2 : 0);
  const __amdgpu_buffer_rsrc_t rWD = RSRC(p.wd, ffn ? DM * FF * 2 : 0);
  u32x4 wgr[8], wur[8], wdr[8];
  {
    const int col = h * 64 + (lane & 7) * 8;
    const int base = col < FF ? (w * 64 + (lane >> 3)) * (FF * 2) + col * 2 : 0x40000000;
#pragma unroll
    for (int i = 0; i < 8; ++i) wgr[i] = BLD(rWG, base, i * 8 * FF * 2, 0);
#pragma unroll
    for (int i = 0; i < 8; ++i) wur[i] = BLD(rWU, base, i * 8 * FF * 2, 0);
#pragma unroll
    for (int i = 0; i < 8; ++i) wdr[i] = BLD(rWD, (h * 64 + w * 8 + i) * 1024 + lane * 16, 0, 0);
  }
  const int colT = w * 64 + (lane & 15) * 4 + ((lane >> 4) & 1) * 2 + (lane >> 5);
  float gaT, sbT, cbT, gbT;
  {
    const gu64* g2 = (const gu64*)(p.ws + WS_G2 + b * 16384);
    u64 g0, g1, g2v, g3;
    for (unsigned spin = 0;; ++spin) {
      g0 = __hip_atomic_load(g2 + colT, RLX_AGENT); g1 = __hip_atomic_load(g2 + 512 + colT, RLX_AGENT);
      g2v = __hip_atomic_load(g2 + 1024 + colT, RLX_AGENT); g3 = __hip_atomic_load(g2 + 1536 + colT, RLX_AGENT);
      const bool ok = (g0 >> 16) == tag && (g1 >> 16) == tag && (g2v >> 16) == tag && (g3 >> 16) == tag;
      if (__all(ok) || spin > SPIN_CAP) break;
      __builtin_amdgcn_s_sleep(8);
    }
    gaT = lo16((u32)g0); sbT = lo16((u32)g1); cbT = lo16((u32)g2v); gbT = lo16((u32)g3);
  }
  const float xT = bf2f(p.x[b * DM + colT]);
  SB();
  STEP2();
  STEP2();
  SB();
  u32 wgp[4][8], wup[4][8], wdp[4][8];
#pragma unroll
  for (int j = 0; j < 4; ++j)
#pragma unroll
    for (int e = 0; e < 4; ++e) {
      wgp[j][2 * e] = pk_lo(wgr[2 * j][e], wgr[2 * j + 1][e]); wgp[j][2 * e + 1] = pk_hi(wgr[2 * j][e], wgr[2 * j + 1][e]);
      wup[j][2 * e] = pk_lo(wur[2 * j][e], wur[2 * j + 1][e]); wup[j][2 * e + 1] = pk_hi(wur[2 * j][e], wur[2 * j + 1][e]);
      wdp[j][2 * e] = pk_lo(wdr[2 * j][e], wdr[2 * j + 1][e]); wdp[j][2 * e + 1] = pk_hi(wdr[2 * j][e], wdr[2 * j + 1][e]);
    }
  SB();
  for (int it = 0; it < NTILE / (2 * NWV) - 5; ++it) { STEP2(); }
  STAMP(6);
  STAMPW(14, 7);
  STAMPW(15, 4);

  {
    const float M = x32_max(x16_max(m));
    const float sc = __builtin_amdgcn_exp2f((m - M) * LOG2E);
    const float L = x32_sum(x16_sum(l * sc));
    float v[8];
#pragma unroll
    for (int j = 0; j < 4; ++j) { v[2 * j] = acc[j][0] * sc; v[2 * j + 1] = acc[j][1] * sc; }
    float r4[4], r2[2];
#pragma unroll
    for (int j = 0; j < 4; ++j) r4[j] = tr16(v[j], v[j + 4]);
#pragma unroll
    for (int j = 0; j < 2; ++j) r2[j] = tr32(r4[j], r4[j + 2]);
    const int dd = (lane & 15) * 8 + ((lane >> 4) & 1) * 4 + (lane >> 5) * 2;
    *(f32x2*)(sMrg + w * 132 + 4 + dd) = f32x2{r2[0], r2[1]};
    if (lane == 0) { sMrg[w * 132] = M; sMrg[w * 132 + 1] = L; }
  }
  BAR();
  {
    float Mw[8], M = -INFINITY;
#pragma unroll
    for (int i = 0; i < 8; ++i) { Mw[i] = sMrg[i * 132]; M = fmaxf(M, Mw[i]); }
    float L = 0.f, o = 0.f;
    const int d = w * 16 + (lane & 15);
#pragma unroll
    for (int i = 0; i < 8; ++i) { const float e = __builtin_amdgcn_exp2f((Mw[i] - M) * LOG2E); L = fmaf(e, sMrg[i * 132 + 1], L); o = fmaf(e, sMrg[i * 132 + 4 + d], o); }
    const u32 ob = f2bf(o / L);
    const u32 nb = __builtin_amdgcn_mov_dpp((int)ob, 0xB1  , 0xf, 0xf, true);
    const u32 op = ob | (nb << 16);
    float a[8] = {0, 0, 0, 0, 0, 0, 0, 0};
#pragma unroll
    for (int j = 0; j < 8; ++j) {
      const u32 oj = __builtin_amdgcn_readlane(op, 2 * j);
      const u32x4 w0 = *(const u32x4*)(smem + L_WO + ((w * 8 + j) * 2) * 1024 + lane * 16);
      const u32x4 w1 = *(const u32x4*)(smem + L_WO + ((w * 8 + j) * 2 + 1) * 1024 + lane * 16);
#pragma unroll
      for (int e = 0; e < 4; ++e) { a[e] = dot2(w0[e], oj, a[e]); a[4 + e] = dot2(w1[e], oj, a[4 + e]); }
    }
    *(f32x4*)(sRed + w * 512 + lane * 8) = f32x4{a[0], a[1], a[2], a[3]};
    *(f32x4*)(sRed + w * 512 + lane * 8 + 4) = f32x4{a[4], a[5], a[6], a[7]};
  }
  BAR();
  if (tid < 128) {
    f32x4 t = {0, 0, 0, 0};
#pragma unroll
    for (int i = 0; i < 8; ++i) t += *(const f32x4*)(sRed + i * 512 + tid * 4);
    const u32x4 u = {clean(t[0]), clean(t[1]), clean(t[2]), clean(t[3])};
    __builtin_amdgcn_raw_buffer_store_b128(u, rPA, h * 2048 + tid * 16, 0, 16);
  }
  STAMP(7);
  const bool fin = h >= FIN0;
  if (!ffn) STAMPFLUSH();
  if (!ffn && !(fin && w == h - FIN0)) return;

  float x2;
  {
    u32x4 g[8];
    const int off = (lane >> 4) * 2048 + w * 256 + (lane & 15) * 16;
    for (unsigned spin = 0;; ++spin) {
      bool ok = true;
#pragma unroll
      for (int i = 0; i < 8; ++i) g[i] = BLD(rPA, off, i * 4 * 2048, 16);
#pragma unroll
      for (int i = 0; i < 8; ++i) ok = ok && g[i][0] != SENT && g[i][1] != SENT && g[i][2] != SENT && g[i][3] != SENT;
      if (__all(ok) || spin > SPIN_CAP) break;
      __builtin_amdgcn_s_sleep(32);
    }
    f32x4 t = {0, 0, 0, 0};
#pragma unroll
    for (int i = 0; i < 8; ++i) t += __builtin_bit_cast(f32x4, g[i]);
    const float r0 = tr16(t[0], t[2]), r1 = tr16(t[1], t[3]);
    const float at = rb(tr32(r0, r1));
    x2 = rb(xT + rb(gaT * at));
  }
  STAMP(8);
  if (ffn) {
    const float y2 = ln_norm(x2, sSt, lane, w);
    const float h2 = rb(rb(y2 * rb(1.0f + cbT)) + sbT);
    {
      const int r = colT & 63;
      ((unsigned short*)(smem + L_HP))[((w * 8 + (r & 7)) * 4 + (r >> 4)) * 2 + ((r >> 3) & 1)] = f2bf(h2);
    }
    LDSWAIT();
    const u32x4 hp = *(const u32x4*)(smem + L_HP + (w * 8 + (lane >> 3)) * 16);
    float ag[8] = {0, 0, 0, 0, 0, 0, 0, 0}, au[8] = {0, 0, 0, 0, 0, 0, 0, 0};
#pragma unroll
    for (int j = 0; j < 4; ++j)
#pragma unroll
      for (int e = 0; e < 8; ++e) { ag[e] = dot2(wgp[j][e], hp[j], ag[e]); au[e] = dot2(wup[j][e], hp[j], au[e]); }
    float r8[8], r4[4];
#pragma unroll
    for (int e = 0; e < 8; ++e) r8[e] = tr16(ag[e], au[e]);
#pragma unroll
    for (int e = 0; e < 4; ++e) r4[e] = tr32(r8[e], r8[e + 4]);
#pragma unroll
    for (int e = 0; e < 4; ++e) r4[e] += dpp<ROR8>(r4[e]);
    if ((lane & 8) == 0) *(f32x4*)(sRed + w * 128 + ((lane >> 4) & 1) * 64 + (lane & 7) * 8 + (lane >> 5) * 4) = f32x4{r4[0], r4[1], r4[2], r4[3]};
    BAR();
    STAMP(9);
    float gs = 0.f, us = 0.f;
    const int f = w * 8 + (lane & 7);
#pragma unroll
    for (int i = 0; i < 8; ++i) { gs += sRed[i * 128 + f]; us += sRed[i * 128 + 64 + f]; }
    gs = rb(gs); us = rb(us);
    const float act = rb(gs / (1.0f + __expf(-gs))) * us;
    const u32 ab = f2bf(act);
    const u32 nb = __builtin_amdgcn_mov_dpp((int)ab, 0xB1, 0xf, 0xf, true);
    const u32 ap = ab | (nb << 16);
    float a[8] = {0, 0, 0, 0, 0, 0, 0, 0};
#pragma unroll
    for (int j = 0; j < 4; ++j) {
      const u32 aj = __builtin_amdgcn_readlane(ap, 2 * j);
#pragma unroll
      for (int e = 0; e < 8; ++e) a[e] = dot2(wdp[j][e], aj, a[e]);
    }
    float* const sRedB = (float*)(smem + L_WO);
    *(f32x4*)(sRedB + w * 512 + lane * 8) = f32x4{a[0], a[1], a[2], a[3]};
    *(f32x4*)(sRedB + w * 512 + lane * 8 + 4) = f32x4{a[4], a[5], a[6], a[7]};
    BAR();
    if (tid < 128) {
      f32x4 t = {0, 0, 0, 0};
#pragma unroll
      for (int i = 0; i < 8; ++i) t += *(const f32x4*)(sRedB + i * 512 + tid * 4);
      const u32x4 u = {clean(t[0]), clean(t[1]), clean(t[2]), clean(t[3])};
      __builtin_amdgcn_raw_buffer_store_b128(u, rPB, h * 2048 + tid * 16, 0, 16);
    }
    STAMP(10);
    STAMPFLUSH();
  } else {
    u32x4 g[6];
    const int off = (lane >> 4) * 2048 + w * 256 + (lane & 15) * 16;
    const bool last_ok = (lane >> 4) < 2;
    for (unsigned spin = 0;; ++spin) {
      bool ok = true;
#pragma unroll
      for (int i = 0; i < 5; ++i) g[i] = BLD(rPB, off, i * 4 * 2048, 16);
      g[5] = BLD(rPB, last_ok ? off : 0x40000000, 5 * 4 * 2048, 16);
#pragma unroll
      for (int i = 0; i < 6; ++i) ok = ok && g[i][0] != SENT && g[i][1] != SENT && g[i][2] != SENT && g[i][3] != SENT;
      if (__all(ok) || spin > SPIN_CAP) break;
      __builtin_amdgcn_s_sleep(8);
    }
    f32x4 t = {0, 0, 0, 0};
#pragma unroll
    for (int i = 0; i < 6; ++i) t += __builtin_bit_cast(f32x4, g[i]);
    const float r0 = tr16(t[0], t[2]), r1 = tr16(t[1], t[3]);
    const float fo = rb(tr32(r0, r1));
    p.out[b * DM + colT] = f2bf(x2 + rb(gbT * fo));
  }
}

extern "C" void kernel_launch(void* const* d_in, const int* in_sizes, int n_in, void* d_out, int out_size, void* d_ws, size_t ws_size,
                              hipStream_t stream) {
  (void)in_sizes; (void)n_in; (void)out_size; (void)ws_size;
  Params p;
  p.x = (const unsigned short*)d_in[0]; p.kc = (const unsigned short*)d_in[1]; p.vc = (const unsigned short*)d_in[2];
  p.wq = (const unsigned short*)d_in[3]; p.wo = (const unsigned short*)d_in[4];
  p.wg = (const unsigned short*)d_in[6]; p.wu = (const unsigned short*)d_in[7]; p.wd = (const unsigned short*)d_in[8];
  p.c = (const unsigned short*)d_in[9]; p.wmod = (const unsigned short*)d_in[10];
  p.out = (unsigned short*)d_out; p.ws = (unsigned char*)d_ws;
  hipFuncSetAttribute((const void*)fused_block_kernel, hipFuncAttributeMaxDynamicSharedMemorySize, LDS_BYTES);
  hipLaunchKernelGGL(fused_block_kernel, dim3(NB * NH), dim3(NT), LDS_BYTES, stream, p);
}
```

```cpp
#include <hip/hip_runtime.h>
#include <hip/hip_bf16.h>
#include <stdint.h>

typedef unsigned int u32;
typedef unsigned long long u64;
using u32x4 = __attribute__((ext_vector_type(4))) unsigned;
using u32x2 = __attribute__((ext_vector_type(2))) unsigned;
using f32x4 = __attribute__((ext_vector_type(4))) float;
using f32x2 = __attribute__((ext_vector_type(2))) float;
using bf16x2 = __attribute__((ext_vector_type(2))) __bf16;
typedef __attribute__((address_space(1))) unsigned long long gu64;
typedef __attribute__((address_space(1))) unsigned gu32;
#define RLX_AGENT __ATOMIC_RELAXED, __HIP_MEMORY_SCOPE_AGENT

constexpr int NB = 8, DM = 512, NH = 32, SK = 4096, DH = 128, FF = 1376;
constexpr int NT = 512, NWV = 8;
constexpr int NFW = 22;
constexpr int FIN0 = 24;
constexpr int TL = 4;
constexpr int NTILE = SK / (4 * TL);
constexpr u32 SENT = 0xFFFFFFFFu;
constexpr u64 TAGMASK = 0xFFFFFFFFFFFFull;
constexpr unsigned SPIN_CAP = 1u << 20;

constexpr size_t WS_EP = 0;
constexpr size_t WS_G1 = 4096;
constexpr size_t WS_G2 = WS_G1 + 8 * 8192;
constexpr size_t WS_PA = WS_G2 + 8 * 16384;
constexpr size_t WS_PB = WS_PA + 8 * 32 * 2048;
constexpr size_t WS_END = WS_PB + 8 * 32 * 2048;
constexpr size_t WS_STAMP = WS_END;

constexpr int L_WO = 0;
constexpr int L_RED = 131072;
constexpr int L_SC = L_RED + 16384;
constexpr int L_HP = L_SC + 2048;
constexpr int L_Q = L_HP + 1024;
constexpr int L_MRG = L_Q + 256;
constexpr int L_ST = L_MRG + 8 * 132 * 4;
constexpr int L_CNT = L_ST + 128;
constexpr int L_RED2 = L_CNT + 16;
constexpr int L_STAMP = L_RED2 + 2048;
constexpr int LDS_BYTES = L_STAMP + 256;

struct Params {
  const unsigned short* x; const unsigned short* kc; const unsigned short* vc; const unsigned short* wq; const unsigned short* wo;
  const unsigned short* wg; const unsigned short* wu; const unsigned short* wd; const unsigned short* c; const unsigned short* wmod;
  unsigned short* out; unsigned char* ws;
};

__device__ __forceinline__ float bf2f(unsigned short v) { return __builtin_bit_cast(float, (u32)v << 16); }
__device__ __forceinline__ unsigned short f2bf(float f) { return __builtin_bit_cast(unsigned short, (__bf16)f); }
__device__ __forceinline__ float rb(float f) { return (float)(__bf16)f; }
__device__ __forceinline__ float lo16(u32 x) { return __builtin_bit_cast(float, x << 16); }
__device__ __forceinline__ float hi16(u32 x) { return __builtin_bit_cast(float, x & 0xffff0000u); }
__device__ __forceinline__ float dot2(u32 a, u32 b, float acc) {
  return __builtin_amdgcn_fdot2_f32_bf16(__builtin_bit_cast(bf16x2, a), __builtin_bit_cast(bf16x2, b), acc, false);
}
template <int CTRL> __device__ __forceinline__ float dpp(float x) {
  return __builtin_bit_cast(float, __builtin_amdgcn_mov_dpp(__builtin_bit_cast(int, x), CTRL, 0xf, 0xf, true));
}
constexpr int ROR1 = 0x121, ROR2 = 0x122, ROR4 = 0x124, ROR8 = 0x128;
__device__ __forceinline__ float row16_sum(float x) { x += dpp<ROR8>(x); x += dpp<ROR4>(x); x += dpp<ROR2>(x); x += dpp<ROR1>(x); return x; }
__device__ __forceinline__ float tr16(float a, float b) {
  auto s = __builtin_amdgcn_permlane16_swap(__builtin_bit_cast(u32, a), __builtin_bit_cast(u32, b), false, false);
  return __uint_as_float(s[0]) + __uint_as_float(s[1]);
}
__device__ __forceinline__ float tr32(float a, float b) {
  auto s = __builtin_amdgcn_permlane32_swap(__builtin_bit_cast(u32, a), __builtin_bit_cast(u32, b), false, false);
  return __uint_as_float(s[0]) + __uint_as_float(s[1]);
}
__device__ __forceinline__ float x16_sum(float x) { return tr16(x, x); }
__device__ __forceinline__ float x32_sum(float x) { return tr32(x, x); }
__device__ __forceinline__ float x16_max(float x) {
  auto s = __builtin_amdgcn_permlane16_swap(__builtin_bit_cast(u32, x), __builtin_bit_cast(u32, x), false, false);
  return fmaxf(__uint_as_float(s[0]), __uint_as_float(s[1]));
}
__device__ __forceinline__ float x32_max(float x) {
  auto s = __builtin_amdgcn_permlane32_swap(__builtin_bit_cast(u32, x), __builtin_bit_cast(u32, x), false, false);
  return fmaxf(__uint_as_float(s[0]), __uint_as_float(s[1]));
}
__device__ __forceinline__ float wave_sum(float x) { return x32_sum(x16_sum(row16_sum(x))); }
__device__ __forceinline__ u32 pk_lo(u32 a, u32 b) { return __builtin_amdgcn_perm(b, a, 0x05040100u); }
__device__ __forceinline__ u32 pk_hi(u32 a, u32 b) { return __builtin_amdgcn_perm(b, a, 0x07060302u); }
__device__ __forceinline__ u32 clean(float v) { u32 u = __builtin_bit_cast(u32, v); return u == SENT ? 0x7FC00000u : u; }

__device__ __forceinline__ u32x2 granule(u64 tag, float v) { const u64 g = (tag << 16) | f2bf(v); return u32x2{(u32)g, (u32)(g >> 32)}; }

#define BAR() do { asm volatile("s_waitcnt lgkmcnt(0)" ::: "memory"); __builtin_amdgcn_s_barrier(); asm volatile("" ::: "memory"); } while (0)
#define LDSWAIT() asm volatile("s_waitcnt lgkmcnt(0)" ::: "memory")
#define CFENCE() asm volatile("" ::: "memory")
#define SB() do { asm volatile("" ::: "memory"); __builtin_amdgcn_sched_barrier(0); } while (0)
#define RSRC(p, n) __builtin_amdgcn_make_buffer_rsrc((void*)(p), 0, (n), 0x00020000)
#define BLD(r, vo, so, aux) __builtin_amdgcn_raw_buffer_load_b128((r), (vo), (so), (aux))

#define STAMP(i) do {} while (0)
#define STAMPW(i, ww) do {} while (0)
#define STAMPFLUSH() do {} while (0)

__device__ __forceinline__ float ln_norm(float val, float* st, int lane, int w) {
  const float sw = wave_sum(val);
  const float mw = sw * (1.0f / 64.0f);
  const float dv = val - mw;
  const float m2 = wave_sum(dv * dv);
  if (lane == 0) { st[2 * w] = sw; st[2 * w + 1] = m2; }
  BAR();
  const f32x4 a = *(const f32x4*)(st), b = *(const f32x4*)(st + 4), c = *(const f32x4*)(st + 8), d = *(const f32x4*)(st + 12);
  const float tot = ((a[0] + a[2]) + (b[0] + b[2])) + ((c[0] + c[2]) + (d[0] + d[2]));
  const float mean = tot * (1.0f / 512.0f);
  float M2 = ((a[1] + a[3]) + (b[1] + b[3])) + ((c[1] + c[3]) + (d[1] + d[3]));
  float e;
  e = a[0] * (1.0f / 64.0f) - mean; float q2 = e * e;
  e = a[2] * (1.0f / 64.0f) - mean; q2 += e * e;
  e = b[0] * (1.0f / 64.0f) - mean; q2 += e * e;
  e = b[2] * (1.0f / 64.0f) - mean; q2 += e * e;
  e = c[0] * (1.0f / 64.0f) - mean; q2 += e * e;
  e = c[2] * (1.0f / 64.0f) - mean; q2 += e * e;
  e = d[0] * (1.0f / 64.0f) - mean; q2 += e * e;
  e = d[2] * (1.0f / 64.0f) - mean; q2 += e * e;
  M2 += 64.0f * q2;
  const float var = rb(M2 * (1.0f / 512.0f));
  const float den = rb(sqrtf(rb(var + 1e-5f)));
  return rb(rb(val - rb(mean)) / den);
}

#define TILEMODE 1
#define NOCLAIM
#define NEXTT(TX, tc) ((TX) + 2 * NWV)
#define TMAP(t) (t)
__global__ void __launch_bounds__(NT, 2) fused_block_kernel(Params p) {
  extern __shared__ __attribute__((aligned(16))) unsigned char smem[];
  const int tid = threadIdx.x, lane = tid & 63;
  const int w = __builtin_amdgcn_readfirstlane(tid >> 6);
  const int b = blockIdx.x >> 5, h = blockIdx.x & 31;
  STAMP(0);

  float* const sRed = (float*)(smem + L_RED);
  float* const sSc = (float*)(smem + L_SC);
  float* const sMrg = (float*)(smem + L_MRG);
  float* const sSt = (float*)(smem + L_ST);
  float* const sRed2 = (float*)(smem + L_RED2);

  const __amdgpu_buffer_rsrc_t rWS = RSRC(p.ws, (int)WS_END);
  const int sPA = (int)WS_PA + b * 32 * 2048, sPB = (int)WS_PB + b * 32 * 2048, sG1 = (int)WS_G1 + b * 8192, sG2 = (int)WS_G2 + b * 16384;
  if (lane < 32) {
    const u32x4 sv = {SENT, SENT, SENT, SENT};
    const int o = h * 2048 + (w & 3) * 512 + lane * 16;
    __builtin_amdgcn_raw_buffer_store_b128(sv, rWS, o, w < 4 ? sPA : sPB, 16);
  }
  CFENCE();
  u64 ep = __hip_atomic_load((gu64*)(p.ws + WS_EP + b * 256), RLX_AGENT);
  const unsigned short cv = p.c[b * DM + tid];
  const unsigned short xv = p.x[b * DM + tid];
  const u32x4 xall = *(const u32x4*)(p.x + b * DM + lane * 8);
  SB();
  const __amdgpu_buffer_rsrc_t rWM = RSRC(p.wmod, DM * 6 * DM * 2);
  const int hs = (h & 7) * 4 + (h >> 3);
  u32x4 wm1[4];
#pragma unroll
  for (int i = 0; i < 4; ++i) wm1[i] = BLD(rWM, (w * 64 + i * 16 + (lane >> 2)) * 6144 + hs * 64 + (lane & 3) * 16, 0, 0);
  SB();
  const __amdgpu_buffer_rsrc_t rK = RSRC(p.kc + (size_t)blockIdx.x * SK * DH, SK * DH * 2);
  const __amdgpu_buffer_rsrc_t rV = RSRC(p.vc + (size_t)blockIdx.x * SK * DH, SK * DH * 2);
  const int vo = lane * 16;
  u32x4 KA[TL], VA[TL], KB[TL], VB[TL];
#define LOADT(dst, r, t) do { _Pragma("unroll") for (int i_ = 0; i_ < TL; ++i_) dst[i_] = BLD(r, vo + i_ * 1024, (t) * (TL * 1024), 2); } while (0)
  LOADT(KA, rK, TMAP(w));
  SB();
  STAMP(16);
  if (tid == 0) { *(u32*)(smem + L_CNT) = 2 * NWV; *(u32*)(smem + L_CNT + 4) = 0; }
  const float cf = bf2f(cv);
  sSc[tid] = rb(cf / (1.0f + __expf(-cf)));
  float y1;
  {
    float xs[8];
#pragma unroll
    for (int j = 0; j < 4; ++j) { xs[2 * j] = lo16(xall[j]); xs[2 * j + 1] = hi16(xall[j]); }
    const float mean = wave_sum(((xs[0] + xs[1]) + (xs[2] + xs[3])) + ((xs[4] + xs[5]) + (xs[6] + xs[7]))) * (1.0f / 512.0f);
    float m2 = 0.f;
#pragma unroll
    for (int j = 0; j < 8; ++j) { const float d = xs[j] - mean; m2 = fmaf(d, d, m2); }
    const float var = rb(wave_sum(m2) * (1.0f / 512.0f));
    const float den = rb(sqrtf(rb(var + 1e-5f)));
    y1 = rb(rb(bf2f(xv) - rb(mean)) / den);
  }
  LDSWAIT();
  STAMP(1);
  {
    float a[8] = {0, 0, 0, 0, 0, 0, 0, 0};
#pragma unroll
    for (int i = 0; i < 4; ++i) {
      const float s = sSc[w * 64 + i * 16 + (lane >> 2)];
#pragma unroll
      for (int j = 0; j < 4; ++j) { a[2 * j] = fmaf(s, lo16(wm1[i][j]), a[2 * j]); a[2 * j + 1] = fmaf(s, hi16(wm1[i][j]), a[2 * j + 1]); }
    }
#pragma unroll
    for (int j = 0; j < 8; ++j) { a[j] += dpp<ROR4>(a[j]); a[j] += dpp<ROR8>(a[j]); }
    float r4[4], r2[2];
#pragma unroll
    for (int j = 0; j < 4; ++j) r4[j] = tr16(a[j], a[j + 4]);
#pragma unroll
    for (int j = 0; j < 2; ++j) r2[j] = tr32(r4[j], r4[j + 2]);
    if ((lane & 12) == 0) {
      const int col = (lane & 3) * 8 + ((lane >> 4) & 1) * 4 + (lane >> 5) * 2;
      *(f32x2*)(sRed + w * 32 + col) = f32x2{r2[0], r2[1]};
    }
  }
  STAMP(17);
  BAR();
  asm volatile("" : "+v"(ep));
  const u64 tag = (ep + 1) & TAGMASK;
  if (tid < 32) {
    float t = 0.f;
#pragma unroll
    for (int i = 0; i < 8; ++i) t += sRed[i * 32 + tid];
    const int col = hs * 32 + tid;
    __builtin_amdgcn_raw_buffer_store_b64(granule(tag, t), rWS, ((col & 511) * 2 + (col >> 9)) * 8, sG1, 16);
  }
  SB();
  STAMP(2);
  const __amdgpu_buffer_rsrc_t rWQ = RSRC(p.wq, DM * NH * DH * 2);
  u32x4 wqr[16];
  {
    const int vq = (w * 64 + (lane >> 4)) * 8192 + h * 256 + (lane & 15) * 16;
#pragma unroll
    for (int i = 0; i < 16; ++i) wqr[i] = BLD(rWQ, vq, ((((i >> 1) + b) & 7) * 8 + (i & 1) * 4) * 8192, 0);
  }
  SB();
  LOADT(VA, rV, TMAP(w));
  SB();
  STAMP(18);
  float hval;
  {
    const u32 tlo = (u32)(tag << 16) >> 16, thi = (u32)(tag >> 16);
    u32x4 g = BLD(rWS, tid * 16, sG1, 16);
    SB();
    LOADT(KB, rK, TMAP(NWV + w)); LOADT(VB, rV, TMAP(NWV + w));
    SB();
    bool ok = (g[0] >> 16) == tlo && g[1] == thi && (g[2] >> 16) == tlo && g[3] == thi;
    if (!__all(ok)) {
      for (unsigned spin = 0; spin < SPIN_CAP; ++spin) {
        if (!ok) g = BLD(rWS, tid * 16, sG1, 16);
        ok = (g[0] >> 16) == tlo && g[1] == thi && (g[2] >> 16) == tlo && g[3] == thi;
        if (__all(ok)) break;
        __builtin_amdgcn_s_sleep(1);
      }
    }
    const float sa = lo16(g[0]), ca = lo16(g[2]);
    hval = rb(rb(y1 * rb(1.0f + ca)) + sa);
  }
  STAMP(4);
  {
    const int r = lane;
    ((unsigned short*)(smem + L_HP))[((w * 4 + (r & 3)) * 8 + (((r >> 3) - b) & 7)) * 2 + ((r >> 2) & 1)] = f2bf(hval);
    LDSWAIT();
    const u32x4 h0 = *(const u32x4*)(smem + L_HP + (w * 4 + (lane >> 4)) * 32), h1 = *(const u32x4*)(smem + L_HP + (w * 4 + (lane >> 4)) * 32 + 16);
    float a[8] = {0, 0, 0, 0, 0, 0, 0, 0};
#pragma unroll
    for (int j = 0; j < 8; ++j) {
      const u32 hp = j < 4 ? h0[j & 3] : h1[j & 3];
#pragma unroll
      for (int e = 0; e < 4; ++e) {
        a[2 * e] = dot2(pk_lo(wqr[2 * j][e], wqr[2 * j + 1][e]), hp, a[2 * e]);
        a[2 * e + 1] = dot2(pk_hi(wqr[2 * j][e], wqr[2 * j + 1][e]), hp, a[2 * e + 1]);
      }
    }
    float r4[4], r2[2];
#pragma unroll
    for (int j = 0; j < 4; ++j) r4[j] = tr16(a[j], a[j + 4]);
#pragma unroll
    for (int j = 0; j < 2; ++j) r2[j] = tr32(r4[j], r4[j + 2]);
    const int col = (lane & 15) * 8 + ((lane >> 4) & 1) * 4 + (lane >> 5) * 2;
    *(f32x2*)(sRed + 256 + w * 128 + col) = f32x2{r2[0], r2[1]};
    BAR();
    if (tid < 128) {
      float t = 0.f;
#pragma unroll
      for (int i = 0; i < 8; ++i) t += sRed[256 + i * 128 + tid];
      ((unsigned short*)(smem + L_Q))[tid] = f2bf(t);
    }
    if (tid == 128 && h == 0) __hip_atomic_store((gu64*)(p.ws + WS_EP + b * 256), tag, RLX_AGENT);
    BAR();
  }
  const u32x4 qv = *(const u32x4*)(smem + L_Q + (lane & 15) * 16);
  STAMP(5);

  float m = -INFINITY, l = 0.f;
  f32x2 acc[4] = {{0, 0}, {0, 0}, {0, 0}, {0, 0}};
  int tA = w, tB = NWV + w;
  const u32 cntA = (u32)(uintptr_t)(__attribute__((address_space(3))) unsigned char*)smem + L_CNT, cntB = cntA + 4;
#define CLAIM(r, addr) do { u64 sv_; asm volatile("s_mov_b64 %1, exec\n\ts_mov_b64 exec, 1\n\tds_add_rtn_u32 %0, %2, %3\n\ts_mov_b64 exec, %1" \
                                                  : "=&v"(r), "=&s"(sv_) : "v"(addr), "v"(1u) : "memory"); } while (0)
  constexpr float SCALE = 0.08838834764831845f, LOG2E = 1.4426950408889634f;
#define CLAIMT(r) do { r = 0; } while (0)
#define STEP(KX, VX, TX) do { \
    u32 tc_; \
    CLAIMT(tc_); \
    SB(); \
    const float bias_ = TX < NTILE ? 0.f : -INFINITY; \
    float s_[TL]; \
    _Pragma("unroll") for (int i_ = 0; i_ < TL; ++i_) { float d_ = dot2(KX[i_][0], qv[0], 0.f); d_ = dot2(KX[i_][1], qv[1], d_); d_ = dot2(KX[i_][2], qv[2], d_); s_[i_] = dot2(KX[i_][3], qv[3], d_); } \
    asm volatile("" : "+v"(s_[0]), "+v"(s_[1]), "+v"(s_[2]), "+v"(s_[3])); \
    asm volatile("s_waitcnt lgkmcnt(0)" : "+v"(tc_)); \
    SB(); \
    TX = NEXTT(TX, tc_); \
    LOADT(KX, rK, TMAP(TX)); \
    SB(); \
    _Pragma("unroll") for (int i_ = 0; i_ < TL; ++i_) s_[i_] = rb(rb(row16_sum(s_[i_])) * SCALE) + bias_; \
    const float mn_ = fmaxf(fmaxf(fmaxf(s_[0], s_[1]), fmaxf(s_[2], s_[3])), m); \
    const float al_ = __builtin_amdgcn_exp2f((m - mn_) * LOG2E); \
    m = mn_; \
    float p_[TL]; \
    _Pragma("unroll") for (int i_ = 0; i_ < TL; ++i_) p_[i_] = __builtin_amdgcn_exp2f((s_[i_] - mn_) * LOG2E); \
    l = fmaf(l, al_, (p_[0] + p_[1]) + (p_[2] + p_[3])); \
    _Pragma("unroll") for (int j_ = 0; j_ < 4; ++j_) acc[j_] *= al_; \
    _Pragma("unroll") for (int i_ = 0; i_ < TL; ++i_) { const f32x2 pp_ = {p_[i_], p_[i_]}; \
      _Pragma("unroll") for (int j_ = 0; j_ < 4; ++j_) acc[j_] = __builtin_elementwise_fma(f32x2{lo16(VX[i_][j_]), hi16(VX[i_][j_])}, pp_, acc[j_]); } \
    asm volatile("" : "+v"(acc[0]), "+v"(acc[1]), "+v"(acc[2]), "+v"(acc[3]), "+v"(m), "+v"(l)); \
    SB(); \
    LOADT(VX, rV, TMAP(TX)); \
    SB(); \
  } while (0)
#define STEP2() do { STEP(KA, VA, tA); STEP(KB, VB, tB); } while (0)

  STEP2();
  SB();
  u32x4 wm2[8];
#pragma unroll
  for (int i = 0; i < 8; ++i) wm2[i] = BLD(rWM, (w * 64 + i * 8 + (lane >> 3)) * 6144 + 2048 + hs * 128 + (lane & 7) * 16, 0, 0);
  SB();
  STEP2();
  SB();
  const __amdgpu_buffer_rsrc_t rWO = RSRC(p.wo, NH * DH * DM * 2);
  u32x4 wor[16];
#pragma unroll
  for (int i = 0; i < 16; ++i) wor[i] = BLD(rWO, (h * 128 + w * 16 + i) * 1024 + lane * 16, 0, 0);
  SB();
  {
    float a[8] = {0, 0, 0, 0, 0, 0, 0, 0};
#pragma unroll
    for (int i = 0; i < 8; ++i) {
      const float s = sSc[w * 64 + i * 8 + (lane >> 3)];
#pragma unroll
      for (int j = 0; j < 4; ++j) { a[2 * j] = fmaf(s, lo16(wm2[i][j]), a[2 * j]); a[2 * j + 1] = fmaf(s, hi16(wm2[i][j]), a[2 * j + 1]); }
    }
#pragma unroll
    for (int j = 0; j < 8; ++j) a[j] += dpp<ROR8>(a[j]);
    float r4[4], r2[2];
#pragma unroll
    for (int j = 0; j < 4; ++j) r4[j] = tr16(a[j], a[j + 4]);
#pragma unroll
    for (int j = 0; j < 2; ++j) r2[j] = tr32(r4[j], r4[j + 2]);
    if ((lane & 8) == 0) {
      const int col = (lane & 7) * 8 + ((lane >> 4) & 1) * 4 + (lane >> 5) * 2;
      *(f32x2*)(sRed2 + w * 64 + col) = f32x2{r2[0], r2[1]};
    }
    LDSWAIT();
    u32 arr;
    CLAIM(arr, cntB);
    asm volatile("s_waitcnt lgkmcnt(0)" : "+v"(arr));
    arr = __builtin_amdgcn_readfirstlane(arr);
    CFENCE();
    if (arr == NWV - 1) {
      float t = 0.f;
#pragma unroll
      for (int i = 0; i < 8; ++i) t += sRed2[i * 64 + lane];
      __builtin_amdgcn_raw_buffer_store_b64(granule(tag, t), rWS, (hs * 64 + lane) * 8, sG2, 16);
    }
  }
  SB();
  STEP2();
  STEP2();
  SB();
#pragma unroll
  for (int j = 0; j < 8; ++j) {
    u32x4 a, c2;
#pragma unroll
    for (int e = 0; e < 2; ++e) { a[2 * e] = pk_lo(wor[2 * j][e], wor[2 * j + 1][e]); a[2 * e + 1] = pk_hi(wor[2 * j][e], wor[2 * j + 1][e]);
                                  c2[2 * e] = pk_lo(wor[2 * j][e + 2], wor[2 * j + 1][e + 2]); c2[2 * e + 1] = pk_hi(wor[2 * j][e + 2], wor[2 * j + 1][e + 2]); }
    *(u32x4*)(smem + L_WO + ((w * 8 + j) * 2) * 1024 + lane * 16) = a;
    *(u32x4*)(smem + L_WO + ((w * 8 + j) * 2 + 1) * 1024 + lane * 16) = c2;
  }
  SB();
  const bool ffn = h < NFW;
  const __amdgpu_buffer_rsrc_t rWG = RSRC(p.wg, ffn ? DM * FF * 2 : 0);
  const __amdgpu_buffer_rsrc_t rWU = RSRC(p.wu, ffn ? DM * FF * 2 : 0);
  const __amdgpu_buffer_rsrc_t rWD = RSRC(p.wd, ffn ? DM * FF * 2 : 0);
  u32x4 wgr[8], wur[8], wdr[8];
  {
    const int col = h * 64 + (lane & 7) * 8;
    const int base = col < FF ? (w * 64 + (lane >> 3)) * (FF * 2) + col * 2 : 0x40000000;
#pragma unroll
    for (int i = 0; i < 8; ++i) wgr[i] = BLD(rWG, base, i * 8 * FF * 2, 0);
#pragma unroll
    for (int i = 0; i < 8; ++i) wur[i] = BLD(rWU, base, i * 8 * FF * 2, 0);
#pragma unroll
    for (int i = 0; i < 8; ++i) wdr[i] = BLD(rWD, (h * 64 + w * 8 + i) * 1024 + lane * 16, 0, 0);
  }
  const int colT = w * 64 + (lane & 15) * 4 + ((lane >> 4) & 1) * 2 + (lane >> 5);
  const float xT = bf2f(p.x[b * DM + colT]);
  SB();
  STEP2();
  STEP2();
  SB();
  u32 wgp[4][8], wup[4][8], wdp[4][8];
#pragma unroll
  for (int j = 0; j < 4; ++j)
#pragma unroll
    for (int e = 0; e < 4; ++e) {
      wgp[j][2 * e] = pk_lo(wgr[2 * j][e], wgr[2 * j + 1][e]); wgp[j][2 * e + 1] = pk_hi(wgr[2 * j][e], wgr[2 * j + 1][e]);
      wup[j][2 * e] = pk_lo(wur[2 * j][e], wur[2 * j + 1][e]); wup[j][2 * e + 1] = pk_hi(wur[2 * j][e], wur[2 * j + 1][e]);
      wdp[j][2 * e] = pk_lo(wdr[2 * j][e], wdr[2 * j + 1][e]); wdp[j][2 * e + 1] = pk_hi(wdr[2 * j][e], wdr[2 * j + 1][e]);
    }
#pragma unroll
  for (int j = 0; j < 4; ++j)
#pragma unroll
    for (int e = 0; e < 8; ++e) asm volatile("" : "+v"(wgp[j][e]), "+v"(wup[j][e]), "+v"(wdp[j][e]));
  SB();
  while (tA < NTILE || tB < NTILE) { STEP2(); }
  SB();
  u32x2 g2v[4];
#pragma unroll
  for (int i = 0; i < 4; ++i) g2v[i] = __builtin_amdgcn_raw_buffer_load_b64(rWS, colT * 8, sG2 + 4096 * i, 16);
  SB();
  STAMP(6);
  STAMPW(14, 7); STAMPW(12, 1); STAMPW(13, 2); STAMPW(3, 3);
  STAMPW(15, 4);

  {
    const float M = x32_max(x16_max(m));
    const float sc = __builtin_amdgcn_exp2f((m - M) * LOG2E);
    const float L = x32_sum(x16_sum(l * sc));
    float v[8];
#pragma unroll
    for (int j = 0; j < 4; ++j) { v[2 * j] = acc[j][0] * sc; v[2 * j + 1] = acc[j][1] * sc; }
    float r4[4], r2[2];
#pragma unroll
    for (int j = 0; j < 4; ++j) r4[j] = tr16(v[j], v[j + 4]);
#pragma unroll
    for (int j = 0; j < 2; ++j) r2[j] = tr32(r4[j], r4[j + 2]);
    const int dd = (lane & 15) * 8 + ((lane >> 4) & 1) * 4 + (lane >> 5) * 2;
    *(f32x2*)(sMrg + w * 132 + 4 + dd) = f32x2{r2[0], r2[1]};
    if (lane == 0) { sMrg[w * 132] = M; sMrg[w * 132 + 1] = L; }
  }
  BAR();
  {
    float Mw[8], M = -INFINITY;
#pragma unroll
    for (int i = 0; i < 8; ++i) { Mw[i] = sMrg[i * 132]; M = fmaxf(M, Mw[i]); }
    float L = 0.f, o = 0.f;
    const int d = w * 16 + (lane & 15);
#pragma unroll
    for (int i = 0; i < 8; ++i) { const float e = __builtin_amdgcn_exp2f((Mw[i] - M) * LOG2E); L = fmaf(e, sMrg[i * 132 + 1], L); o = fmaf(e, sMrg[i * 132 + 4 + d], o); }
    const u32 ob = f2bf(o / L);
    const u32 nb = __builtin_amdgcn_mov_dpp((int)ob, 0xB1  , 0xf, 0xf, true);
    const u32 op = ob | (nb << 16);
    float a[8] = {0, 0, 0, 0, 0, 0, 0, 0};
#pragma unroll
    for (int j = 0; j < 8; ++j) {
      const u32 oj = __builtin_amdgcn_readlane(op, 2 * j);
      const u32x4 w0 = *(const u32x4*)(smem + L_WO + ((w * 8 + j) * 2) * 1024 + lane * 16);
      const u32x4 w1 = *(const u32x4*)(smem + L_WO + ((w * 8 + j) * 2 + 1) * 1024 + lane * 16);
#pragma unroll
      for (int e = 0; e < 4; ++e) { a[e] = dot2(w0[e], oj, a[e]); a[4 + e] = dot2(w1[e], oj, a[4 + e]); }
    }
    *(f32x4*)(sRed + w * 512 + lane * 8) = f32x4{a[0], a[1], a[2], a[3]};
    *(f32x4*)(sRed + w * 512 + lane * 8 + 4) = f32x4{a[4], a[5], a[6], a[7]};
  }
  BAR();
  if (tid < 128) {
    f32x4 t = {0, 0, 0, 0};
#pragma unroll
    for (int i = 0; i < 8; ++i) t += *(const f32x4*)(sRed + i * 512 + tid * 4);
    const u32x4 u = {clean(t[0]), clean(t[1]), clean(t[2]), clean(t[3])};
    __builtin_amdgcn_raw_buffer_store_b128(u, rWS, h * 2048 + tid * 16, sPA, 16);
  }
  STAMP(7);
  const bool fin = h >= FIN0;
  if (!ffn) STAMPFLUSH();
  if (!ffn && !(fin && w == h - FIN0)) return;

  float x2, sbT, cbT, gbT;
  {
    u32x4 g[8];
    const int off = (lane >> 4) * 2048 + w * 256 + (lane & 15) * 16;
#pragma unroll
    for (int i = 0; i < 8; ++i) g[i] = BLD(rWS, off, sPA + i * 4 * 2048, 16);
    for (unsigned spin = 0;; ++spin) {
      bool ok = true;
#pragma unroll
      for (int i = 0; i < 8; ++i) {
        const bool oki = g[i][0] != SENT && g[i][1] != SENT && g[i][2] != SENT && g[i][3] != SENT;
        ok = ok && oki;
        if (!oki) g[i] = BLD(rWS, off, sPA + i * 4 * 2048, 16);
      }
      if (__all(ok) || spin > SPIN_CAP) break;
      __builtin_amdgcn_s_sleep(2);
    }
    {
      const u32 tlo = (u32)(tag << 16) >> 16, thi = (u32)(tag >> 16);
      for (unsigned spin = 0;; ++spin) {
        bool ok = true;
#pragma unroll
        for (int i = 0; i < 4; ++i) ok = ok && (g2v[i][0] >> 16) == tlo && g2v[i][1] == thi;
        if (__all(ok) || spin > SPIN_CAP) break;
        __builtin_amdgcn_s_sleep(2);
#pragma unroll
        for (int i = 0; i < 4; ++i) g2v[i] = __builtin_amdgcn_raw_buffer_load_b64(rWS, colT * 8, sG2 + 4096 * i, 16);
      }
    }
    const float gaT = lo16(g2v[0][0]);
    sbT = lo16(g2v[1][0]); cbT = lo16(g2v[2][0]); gbT = lo16(g2v[3][0]);
    f32x4 t = {0, 0, 0, 0};
#pragma unroll
    for (int i = 0; i < 8; ++i) t += __builtin_bit_cast(f32x4, g[i]);
    const float r0 = tr16(t[0], t[2]), r1 = tr16(t[1], t[3]);
    const float at = rb(tr32(r0, r1));
    x2 = rb(xT + rb(gaT * at));
  }
  STAMP(8);
  if (ffn) {
    const float y2 = ln_norm(x2, sSt, lane, w);
    const float h2 = rb(rb(y2 * rb(1.0f + cbT)) + sbT);
    {
      const int r = colT & 63;
      ((unsigned short*)(smem + L_HP))[((w * 8 + (r & 7)) * 4 + (r >> 4)) * 2 + ((r >> 3) & 1)] = f2bf(h2);
    }
    LDSWAIT();
    const u32x4 hp = *(const u32x4*)(smem + L_HP + (w * 8 + (lane >> 3)) * 16);
    float ag[8] = {0, 0, 0, 0, 0, 0, 0, 0}, au[8] = {0, 0, 0, 0, 0, 0, 0, 0};
#pragma unroll
    for (int j = 0; j < 4; ++j)
#pragma unroll
      for (int e = 0; e < 8; ++e) { ag[e] = dot2(wgp[j][e], hp[j], ag[e]); au[e] = dot2(wup[j][e], hp[j], au[e]); }
    float r8[8], r4[4];
#pragma unroll
    for (int e = 0; e < 8; ++e) r8[e] = tr16(ag[e], au[e]);
#pragma unroll
    for (int e = 0; e < 4; ++e) r4[e] = tr32(r8[e], r8[e + 4]);
#pragma unroll
    for (int e = 0; e < 4; ++e) r4[e] += dpp<ROR8>(r4[e]);
    if ((lane & 8) == 0) *(f32x4*)(sRed + w * 128 + ((lane >> 4) & 1) * 64 + (lane & 7) * 8 + (lane >> 5) * 4) = f32x4{r4[0], r4[1], r4[2], r4[3]};
    BAR();
    STAMP(9);
    float gs = 0.f, us = 0.f;
    const int f = w * 8 + (lane & 7);
#pragma unroll
    for (int i = 0; i < 8; ++i) { gs += sRed[i * 128 + f]; us += sRed[i * 128 + 64 + f]; }
    gs = rb(gs); us = rb(us);
    const float act = rb(gs / (1.0f + __expf(-gs))) * us;
    const u32 ab = f2bf(act);
    const u32 nb = __builtin_amdgcn_mov_dpp((int)ab, 0xB1, 0xf, 0xf, true);
    const u32 ap = ab | (nb << 16);
    float a[8] = {0, 0, 0, 0, 0, 0, 0, 0};
#pragma unroll
    for (int j = 0; j < 4; ++j) {
      const u32 aj = __builtin_amdgcn_readlane(ap, 2 * j);
#pragma unroll
      for (int e = 0; e < 8; ++e) a[e] = dot2(wdp[j][e], aj, a[e]);
    }
    float* const sRedB = (float*)(smem + L_WO);
    *(f32x4*)(sRedB + w * 512 + lane * 8) = f32x4{a[0], a[1], a[2], a[3]};
    *(f32x4*)(sRedB + w * 512 + lane * 8 + 4) = f32x4{a[4], a[5], a[6], a[7]};
    BAR();
    if (tid < 128) {
      f32x4 t = {0, 0, 0, 0};
#pragma unroll
      for (int i = 0; i < 8; ++i) t += *(const f32x4*)(sRedB + i * 512 + tid * 4);
      const u32x4 u = {clean(t[0]), clean(t[1]), clean(t[2]), clean(t[3])};
      __builtin_amdgcn_raw_buffer_store_b128(u, rWS, h * 2048 + tid * 16, sPB, 16);
    }
    STAMP(10);
    STAMPFLUSH();
  } else {
    u32x4 g[6];
    const int off = (lane >> 4) * 2048 + w * 256 + (lane & 15) * 16;
    const bool last_ok = (lane >> 4) < 2;
#pragma unroll
    for (int i = 0; i < 5; ++i) g[i] = BLD(rWS, off, sPB + i * 4 * 2048, 16);
    g[5] = BLD(rWS, last_ok ? off : 0x40000000, sPB + 5 * 4 * 2048, 16);
    for (unsigned spin = 0;; ++spin) {
      bool ok = true;
#pragma unroll
      for (int i = 0; i < 6; ++i) {
        const bool oki = g[i][0] != SENT && g[i][1] != SENT && g[i][2] != SENT && g[i][3] != SENT;
        ok = ok && oki;
        if (!oki) g[i] = BLD(rWS, off, sPB + i * 4 * 2048, 16);
      }
      if (__all(ok) || spin > SPIN_CAP) break;
      __builtin_amdgcn_s_sleep(1);
    }
    f32x4 t = {0, 0, 0, 0};
#pragma unroll
    for (int i = 0; i < 6; ++i) t += __builtin_bit_cast(f32x4, g[i]);
    const float r0 = tr16(t[0], t[2]), r1 = tr16(t[1], t[3]);
    const float fo = rb(tr32(r0, r1));
    p.out[b * DM + colT] = f2bf(x2 + rb(gbT * fo));
  }
}

extern "C" void kernel_launch(void* const* d_in, const int* in_sizes, int n_in, void* d_out, int out_size, void* d_ws, size_t ws_size,
                              hipStream_t stream) {
  (void)in_sizes; (void)n_in; (void)out_size; (void)ws_size;
  Params p;
  p.x = (const unsigned short*)d_in[0]; p.kc = (const unsigned short*)d_in[1]; p.vc = (const unsigned short*)d_in[2];
  p.wq = (const unsigned short*)d_in[3]; p.wo = (const unsigned short*)d_in[4];
  p.wg = (const unsigned short*)d_in[6]; p.wu = (const unsigned short*)d_in[7]; p.wd = (const unsigned short*)d_in[8];
  p.c = (const unsigned short*)d_in[9]; p.wmod = (const unsigned short*)d_in[10];
  p.out = (unsigned short*)d_out; p.ws = (unsigned char*)d_ws;
  hipFuncSetAttribute((const void*)fused_block_kernel, hipFuncAttributeMaxDynamicSharedMemorySize, LDS_BYTES);
  hipLaunchKernelGGL(fused_block_kernel, dim3(NB * NH), dim3(NT), LDS_BYTES, stream, p);
}
```

```cpp
#include <hip/hip_runtime.h>
#include <hip/hip_bf16.h>
#include <stdint.h>

typedef unsigned int u32;
typedef unsigned long long u64;
using u32x4 = __attribute__((ext_vector_type(4))) unsigned;
using u32x2 = __attribute__((ext_vector_type(2))) unsigned;
using f32x4 = __attribute__((ext_vector_type(4))) float;
using bf16x8 = __attribute__((ext_vector_type(8))) __bf16;
using s16x4 = __attribute__((ext_vector_type(4))) short;
using f32x2 = __attribute__((ext_vector_type(2))) float;
using bf16x2 = __attribute__((ext_vector_type(2))) __bf16;
typedef __attribute__((address_space(1))) unsigned long long gu64;
typedef __attribute__((address_space(1))) unsigned gu32;
#define RLX_AGENT __ATOMIC_RELAXED, __HIP_MEMORY_SCOPE_AGENT

constexpr int NB = 8, DM = 512, NH = 32, SK = 4096, DH = 128, FF = 1376;
constexpr int NT = 512, NWV = 8;
constexpr int NFW = FF / 16;
constexpr int TL = 4;
constexpr int NTILE = SK / (4 * TL);
constexpr u32 SENT = 0xFFFFFFFFu;
constexpr u64 TAGMASK = 0xFFFFFFFFFFFFull;
constexpr unsigned SPIN_CAP = 1u << 20;

constexpr size_t WS_EP = 0;
constexpr size_t WS_FL = 256;
constexpr size_t WS_G1 = 4096;
constexpr size_t WS_G2 = WS_G1 + 8 * 8192;
constexpr size_t WS_PA = WS_G2 + 8 * 16384;
constexpr size_t WS_X2 = WS_PA + 8 * 32 * 2048;
constexpr size_t WS_PB = WS_X2 + 8 * 4096;
constexpr size_t WS_END = WS_PB + (size_t)NFW * 16384;
constexpr size_t WS_STAMP = WS_END;

constexpr int L_WO = 0;
constexpr int L_RED = 131072;
constexpr int L_SC = L_RED + 16384;
constexpr int L_HP = L_SC + 2048;
constexpr int L_Q = L_HP + 1024;
constexpr int L_MRG = L_Q + 256;
constexpr int L_CNT = L_MRG + 8 * 132 * 4;
constexpr int L_RED2 = L_CNT + 16;
constexpr int L_STAMP = L_RED2 + 2048;
constexpr int LDS_BYTES = L_STAMP + 256;

struct Params {
  const unsigned short* x; const unsigned short* kc; const unsigned short* vc; const unsigned short* wq; const unsigned short* wo;
  const unsigned short* wg; const unsigned short* wu; const unsigned short* wd; const unsigned short* c; const unsigned short* wmod;
  unsigned short* out; unsigned char* ws;
};

__device__ __forceinline__ float bf2f(unsigned short v) { return __builtin_bit_cast(float, (u32)v << 16); }
__device__ __forceinline__ unsigned short f2bf(float f) { return __builtin_bit_cast(unsigned short, (__bf16)f); }
__device__ __forceinline__ float rb(float f) { return (float)(__bf16)f; }
__device__ __forceinline__ float lo16(u32 x) { return __builtin_bit_cast(float, x << 16); }
__device__ __forceinline__ float hi16(u32 x) { return __builtin_bit_cast(float, x & 0xffff0000u); }
__device__ __forceinline__ float dot2(u32 a, u32 b, float acc) {
  return __builtin_amdgcn_fdot2_f32_bf16(__builtin_bit_cast(bf16x2, a), __builtin_bit_cast(bf16x2, b), acc, false);
}
template <int CTRL> __device__ __forceinline__ float dpp(float x) {
  return __builtin_bit_cast(float, __builtin_amdgcn_mov_dpp(__builtin_bit_cast(int, x), CTRL, 0xf, 0xf, true));
}
constexpr int ROR1 = 0x121, ROR2 = 0x122, ROR4 = 0x124, ROR8 = 0x128;
__device__ __forceinline__ float row16_sum(float x) { x += dpp<ROR8>(x); x += dpp<ROR4>(x); x += dpp<ROR2>(x); x += dpp<ROR1>(x); return x; }
__device__ __forceinline__ float tr16(float a, float b) {
  auto s = __builtin_amdgcn_permlane16_swap(__builtin_bit_cast(u32, a), __builtin_bit_cast(u32, b), false, false);
  return __uint_as_float(s[0]) + __uint_as_float(s[1]);
}
__device__ __forceinline__ float tr32(float a, float b) {
  auto s = __builtin_amdgcn_permlane32_swap(__builtin_bit_cast(u32, a), __builtin_bit_cast(u32, b), false, false);
  return __uint_as_float(s[0]) + __uint_as_float(s[1]);
}
__device__ __forceinline__ float x16_sum(float x) { return tr16(x, x); }
__device__ __forceinline__ float x32_sum(float x) { return tr32(x, x); }
__device__ __forceinline__ float x16_max(float x) {
  auto s = __builtin_amdgcn_permlane16_swap(__builtin_bit_cast(u32, x), __builtin_bit_cast(u32, x), false, false);
  return fmaxf(__uint_as_float(s[0]), __uint_as_float(s[1]));
}
__device__ __forceinline__ float x32_max(float x) {
  auto s = __builtin_amdgcn_permlane32_swap(__builtin_bit_cast(u32, x), __builtin_bit_cast(u32, x), false, false);
  return fmaxf(__uint_as_float(s[0]), __uint_as_float(s[1]));
}
__device__ __forceinline__ float wave_sum(float x) { return x32_sum(x16_sum(row16_sum(x))); }
__device__ __forceinline__ u32 pk_lo(u32 a, u32 b) { return __builtin_amdgcn_perm(b, a, 0x05040100u); }
__device__ __forceinline__ u32 pk_hi(u32 a, u32 b) { return __builtin_amdgcn_perm(b, a, 0x07060302u); }
__device__ __forceinline__ u32 clean(float v) { u32 u = __builtin_bit_cast(u32, v); return u == SENT ? 0x7FC00000u : u; }

__device__ __forceinline__ u32x2 granule(u64 tag, float v) { const u64 g = (tag << 16) | f2bf(v); return u32x2{(u32)g, (u32)(g >> 32)}; }

#define BAR() do { asm volatile("s_waitcnt lgkmcnt(0)" ::: "memory"); __builtin_amdgcn_s_barrier(); asm volatile("" ::: "memory"); } while (0)
#define LDSWAIT() asm volatile("s_waitcnt lgkmcnt(0)" ::: "memory")
#define CFENCE() asm volatile("" ::: "memory")
#define SB() do { asm volatile("" ::: "memory"); __builtin_amdgcn_sched_barrier(0); } while (0)
#define RSRC(p, n) __builtin_amdgcn_make_buffer_rsrc((void*)(p), 0, (n), 0x00020000)
#define BLD(r, vo, so, aux) __builtin_amdgcn_raw_buffer_load_b128((r), (vo), (so), (aux))

#define STAMP(i) do {} while (0)
#define STAMPW(i, ww) do {} while (0)
#define STAMPFLUSH() do {} while (0)

__device__ __forceinline__ void ln_stats(const float* xs, float& meanb, float& den) {
  const float mean = wave_sum(((xs[0] + xs[1]) + (xs[2] + xs[3])) + ((xs[4] + xs[5]) + (xs[6] + xs[7]))) * (1.0f / 512.0f);
  float m2 = 0.f;
#pragma unroll
  for (int j = 0; j < 8; ++j) { const float d = xs[j] - mean; m2 = fmaf(d, d, m2); }
  const float var = rb(wave_sum(m2) * (1.0f / 512.0f));
  den = rb(sqrtf(rb(var + 1e-5f)));
  meanb = rb(mean);
}
__device__ __forceinline__ bool has_sent(u32x4 g) { return g[0] == SENT || g[1] == SENT || g[2] == SENT || g[3] == SENT; }

#define HW h
#define WAUX 0
#define G2IT 3
#define A1SLP 2
#define BSLP 24
#define XSLP 12
#define HEADV 1
#define NPRE 2
#define TILEMODE 1
#define NOCLAIM
#define NEXTT(TX, tc) (tn += NWV)
#define TMAP(t) (t)
__global__ void __launch_bounds__(NT, 2) fused_block_kernel(Params p) {
  extern __shared__ __attribute__((aligned(16))) unsigned char smem[];
  const int tid = threadIdx.x, lane = tid & 63;
  const int w = __builtin_amdgcn_readfirstlane(tid >> 6);
  const int b = blockIdx.x >> 5, h = blockIdx.x & 31;
  STAMP(0);

  float* const sRed = (float*)(smem + L_RED);
  float* const sSc = (float*)(smem + L_SC);
  float* const sMrg = (float*)(smem + L_MRG);
  float* const sRed2 = (float*)(smem + L_RED2);

  const __amdgpu_buffer_rsrc_t rWS = RSRC(p.ws, (int)WS_END);
  const int g = blockIdx.x;
  const int fw = (g & 7) * 11 + (g >> 3) / 3;
  const bool ffn = (g >> 3) % 3 == 0 && fw < NFW;
  const int sPA = (int)WS_PA + b * 32 * 2048, sG1 = (int)WS_G1 + b * 8192, sG2 = (int)WS_G2 + b * 16384;
  {
    const u32x4 sv = {SENT, SENT, SENT, SENT};
    if (tid < 128) __builtin_amdgcn_raw_buffer_store_b128(sv, rWS, tid * 16, sPA + h * 2048, 16);
    else if (tid < 136) __builtin_amdgcn_raw_buffer_store_b128(sv, rWS, (tid - 128) * 16, (int)WS_X2 + b * 4096 + h * 128, 16);
    if (ffn) {
      __builtin_amdgcn_raw_buffer_store_b128(sv, rWS, tid * 16, (int)WS_PB + fw * 16384, 16);
      __builtin_amdgcn_raw_buffer_store_b128(sv, rWS, tid * 16, (int)WS_PB + fw * 16384 + 8192, 16);
    }
  }
  CFENCE();
  u64 ep = __hip_atomic_load((gu64*)(p.ws + WS_EP), RLX_AGENT);
  const unsigned short cv = p.c[b * DM + tid];
  const unsigned short xv = p.x[b * DM + tid];
  const u32x4 xall = *(const u32x4*)(p.x + b * DM + lane * 8);
  SB();
  const __amdgpu_buffer_rsrc_t rWM = RSRC(p.wmod, DM * 6 * DM * 2);
  const int hs = (h & 7) * 4 + (h >> 3);
  u32x4 wm1[4];
#pragma unroll
  for (int i = 0; i < 4; ++i) wm1[i] = BLD(rWM, (w * 64 + i * 16 + (lane >> 2)) * 6144 + hs * 64 + (lane & 3) * 16, 0, 0);
  SB();
  const __amdgpu_buffer_rsrc_t rK = RSRC(p.kc + (size_t)blockIdx.x * SK * DH, SK * DH * 2);
  const __amdgpu_buffer_rsrc_t rV = RSRC(p.vc + (size_t)blockIdx.x * SK * DH, SK * DH * 2);
  const int vo = lane * 16;
  u32x4 KA[TL], VA[TL], KB[TL], VB[TL];
#define LOADT(dst, r, t) do { _Pragma("unroll") for (int i_ = 0; i_ < TL; ++i_) dst[i_] = BLD(r, vo + i_ * 1024, (t) * (TL * 1024), 2); } while (0)
  LOADT(KA, rK, TMAP(w));
  SB();
  STAMP(16);
  if (tid == 0) { *(u32*)(smem + L_CNT) = NPRE * NWV; *(u32*)(smem + L_CNT + 4) = 0; }
  const float cf = bf2f(cv);
  sSc[tid] = rb(cf / (1.0f + __expf(-cf)));
  float y1;
  {
    float xs[8];
#pragma unroll
    for (int j = 0; j < 4; ++j) { xs[2 * j] = lo16(xall[j]); xs[2 * j + 1] = hi16(xall[j]); }
    float meanb, den;
    ln_stats(xs, meanb, den);
    y1 = rb(rb(bf2f(xv) - meanb) / den);
  }
  LDSWAIT();
  STAMP(1);
  {
    float a[8] = {0, 0, 0, 0, 0, 0, 0, 0};
#pragma unroll
    for (int i = 0; i < 4; ++i) {
      const float s = sSc[w * 64 + i * 16 + (lane >> 2)];
#pragma unroll
      for (int j = 0; j < 4; ++j) { a[2 * j] = fmaf(s, lo16(wm1[i][j]), a[2 * j]); a[2 * j + 1] = fmaf(s, hi16(wm1[i][j]), a[2 * j + 1]); }
    }
#pragma unroll
    for (int j = 0; j < 8; ++j) { a[j] += dpp<ROR4>(a[j]); a[j] += dpp<ROR8>(a[j]); }
    float r4[4], r2[2];
#pragma unroll
    for (int j = 0; j < 4; ++j) r4[j] = tr16(a[j], a[j + 4]);
#pragma unroll
    for (int j = 0; j < 2; ++j) r2[j] = tr32(r4[j], r4[j + 2]);
    if ((lane & 12) == 0) {
      const int col = (lane & 3) * 8 + ((lane >> 4) & 1) * 4 + (lane >> 5) * 2;
      *(f32x2*)(sRed + w * 32 + col) = f32x2{r2[0], r2[1]};
    }
  }
  STAMP(17);
  BAR();
  asm volatile("" : "+v"(ep));
  const u64 tag = (ep + 1) & TAGMASK;
  if (tid < 32) {
    float t = 0.f;
#pragma unroll
    for (int i = 0; i < 8; ++i) t += sRed[i * 32 + tid];
    const int col = hs * 32 + tid;
    __builtin_amdgcn_raw_buffer_store_b64(granule(tag, t), rWS, ((col & 511) * 2 + (col >> 9)) * 8, sG1, 16);
  }
  SB();
  STAMP(2);
  const __amdgpu_buffer_rsrc_t rWQ = RSRC(p.wq, DM * NH * DH * 2);
  u32x4 wqr[16];
  {
    const int vq = (w * 64 + (lane >> 4)) * 8192 + h * 256 + (lane & 15) * 16;
#pragma unroll
    for (int i = 0; i < 16; ++i) wqr[i] = BLD(rWQ, vq, ((((i >> 1) + b) & 7) * 8 + (i & 1) * 4) * 8192, 0);
  }
  SB();
  SB();
  STAMP(18);
  float hval;
  {
    const u32 tlo = (u32)(tag << 16) >> 16, thi = (u32)(tag >> 16);
    u32x4 g = BLD(rWS, tid * 16, sG1, 16);
    SB();
    __builtin_amdgcn_s_barrier();
    SB();
    LOADT(VA, rV, TMAP(w));
    LOADT(KB, rK, TMAP(NWV + w)); LOADT(VB, rV, TMAP(NWV + w));
    SB();
    bool ok = (g[0] >> 16) == tlo && g[1] == thi && (g[2] >> 16) == tlo && g[3] == thi;
    if (!__all(ok)) {
      for (unsigned spin = 0; spin < SPIN_CAP; ++spin) {
        if (!ok) g = BLD(rWS, tid * 16, sG1, 16);
        ok = (g[0] >> 16) == tlo && g[1] == thi && (g[2] >> 16) == tlo && g[3] == thi;
        if (__all(ok)) break;
        __builtin_amdgcn_s_sleep(1);
      }
    }
    const float sa = lo16(g[0]), ca = lo16(g[2]);
    hval = rb(rb(y1 * rb(1.0f + ca)) + sa);
  }
  STAMP(4);
  {
    const int r = lane;
    ((unsigned short*)(smem + L_HP))[((w * 4 + (r & 3)) * 8 + (((r >> 3) - b) & 7)) * 2 + ((r >> 2) & 1)] = f2bf(hval);
    LDSWAIT();
    const u32x4 h0 = *(const u32x4*)(smem + L_HP + (w * 4 + (lane >> 4)) * 32), h1 = *(const u32x4*)(smem + L_HP + (w * 4 + (lane >> 4)) * 32 + 16);
    float a[8] = {0, 0, 0, 0, 0, 0, 0, 0};
#pragma unroll
    for (int j = 0; j < 8; ++j) {
      const u32 hp = j < 4 ? h0[j & 3] : h1[j & 3];
#pragma unroll
      for (int e = 0; e < 4; ++e) {
        a[2 * e] = dot2(pk_lo(wqr[2 * j][e], wqr[2 * j + 1][e]), hp, a[2 * e]);
        a[2 * e + 1] = dot2(pk_hi(wqr[2 * j][e], wqr[2 * j + 1][e]), hp, a[2 * e + 1]);
      }
    }
    float r4[4], r2[2];
#pragma unroll
    for (int j = 0; j < 4; ++j) r4[j] = tr16(a[j], a[j + 4]);
#pragma unroll
    for (int j = 0; j < 2; ++j) r2[j] = tr32(r4[j], r4[j + 2]);
    const int col = (lane & 15) * 8 + ((lane >> 4) & 1) * 4 + (lane >> 5) * 2;
    *(f32x2*)(sRed + 256 + w * 128 + col) = f32x2{r2[0], r2[1]};
    BAR();
    if (tid < 128) {
      float t = 0.f;
#pragma unroll
      for (int i = 0; i < 8; ++i) t += sRed[256 + i * 128 + tid];
      ((unsigned short*)(smem + L_Q))[tid] = f2bf(t);
    }
    if (tid == 128 && h == 0) __hip_atomic_store((gu64*)(p.ws + WS_FL) + b, tag, RLX_AGENT);
    BAR();
  }
  const u32x4 qv = *(const u32x4*)(smem + L_Q + (lane & 15) * 16);
  STAMP(5);

  float m = -INFINITY, l = 0.f;
  f32x2 acc[4] = {{0, 0}, {0, 0}, {0, 0}, {0, 0}};
  int tn = (NPRE - 1) * NWV + w; (void)tn;
  int tA = w, tB = NWV + w;
  const u32 cntA = (u32)(uintptr_t)(__attribute__((address_space(3))) unsigned char*)smem + L_CNT, cntB = cntA + 4;
#define CLAIM(r, addr) do { u64 sv_; asm volatile("s_mov_b64 %1, exec\n\ts_mov_b64 exec, 1\n\tds_add_rtn_u32 %0, %2, %3\n\ts_mov_b64 exec, %1" \
                                                  : "=&v"(r), "=&s"(sv_) : "v"(addr), "v"(1u) : "memory"); } while (0)
  constexpr float SCALE = 0.08838834764831845f, LOG2E = 1.4426950408889634f;
#define CLAIMT(r) do { r = 0; } while (0)
#define WAITT(r) do { } while (0)
#define STEPR(KX, VX, TX, RELOAD) do { \
    u32 tc_ = 0; \
    if (RELOAD) CLAIMT(tc_); \
    SB(); \
    const float bias_ = TX < NTILE ? 0.f : -INFINITY; \
    float s_[TL]; \
    _Pragma("unroll") for (int i_ = 0; i_ < TL; ++i_) { float d_ = dot2(KX[i_][0], qv[0], 0.f); d_ = dot2(KX[i_][1], qv[1], d_); d_ = dot2(KX[i_][2], qv[2], d_); s_[i_] = dot2(KX[i_][3], qv[3], d_); } \
    asm volatile("" : "+v"(s_[0]), "+v"(s_[1]), "+v"(s_[2]), "+v"(s_[3])); \
    if (RELOAD) WAITT(tc_); \
    SB(); \
    if (RELOAD) { TX = NEXTT(TX, tc_); LOADT(KX, rK, TMAP(TX)); } \
    SB(); \
    _Pragma("unroll") for (int i_ = 0; i_ < TL; ++i_) s_[i_] = rb(rb(row16_sum(s_[i_])) * SCALE) + bias_; \
    const float mn_ = fmaxf(fmaxf(fmaxf(s_[0], s_[1]), fmaxf(s_[2], s_[3])), m); \
    const float al_ = __builtin_amdgcn_exp2f((m - mn_) * LOG2E); \
    m = mn_; \
    float p_[TL]; \
    _Pragma("unroll") for (int i_ = 0; i_ < TL; ++i_) p_[i_] = __builtin_amdgcn_exp2f((s_[i_] - mn_) * LOG2E); \
    l = fmaf(l, al_, (p_[0] + p_[1]) + (p_[2] + p_[3])); \
    _Pragma("unroll") for (int j_ = 0; j_ < 4; ++j_) acc[j_] *= al_; \
    _Pragma("unroll") for (int i_ = 0; i_ < TL; ++i_) { const f32x2 pp_ = {p_[i_], p_[i_]}; \
      _Pragma("unroll") for (int j_ = 0; j_ < 4; ++j_) acc[j_] = __builtin_elementwise_fma(f32x2{lo16(VX[i_][j_]), hi16(VX[i_][j_])}, pp_, acc[j_]); } \
    asm volatile("" : "+v"(acc[0]), "+v"(acc[1]), "+v"(acc[2]), "+v"(acc[3]), "+v"(m), "+v"(l)); \
    SB(); \
    if (RELOAD) LOADT(VX, rV, TMAP(TX)); \
    SB(); \
  } while (0)
#define STEP(KX, VX, TX) STEPR(KX, VX, TX, true)
#define STEP2() do { STEP(KA, VA, tA); STEP(KB, VB, tB); } while (0)

  STEP2();
  SB();
  u32x4 wm2[8];
#pragma unroll
  for (int i = 0; i < 8; ++i) wm2[i] = BLD(rWM, (w * 64 + i * 8 + (lane >> 3)) * 6144 + 2048 + hs * 128 + (lane & 7) * 16, 0, 0);
  SB();
  STEP2();
  SB();
  const __amdgpu_buffer_rsrc_t rWO = RSRC(p.wo, NH * DH * DM * 2);
  u32x4 wor[16];
#pragma unroll
  for (int i = 0; i < 16; ++i) wor[i] = BLD(rWO, (HW * 128 + w * 16 + i) * 1024 + lane * 16, 0, WAUX);
  SB();
  {
    float a[8] = {0, 0, 0, 0, 0, 0, 0, 0};
#pragma unroll
    for (int i = 0; i < 8; ++i) {
      const float s = sSc[w * 64 + i * 8 + (lane >> 3)];
#pragma unroll
      for (int j = 0; j < 4; ++j) { a[2 * j] = fmaf(s, lo16(wm2[i][j]), a[2 * j]); a[2 * j + 1] = fmaf(s, hi16(wm2[i][j]), a[2 * j + 1]); }
    }
#pragma unroll
    for (int j = 0; j < 8; ++j) a[j] += dpp<ROR8>(a[j]);
    float r4[4], r2[2];
#pragma unroll
    for (int j = 0; j < 4; ++j) r4[j] = tr16(a[j], a[j + 4]);
#pragma unroll
    for (int j = 0; j < 2; ++j) r2[j] = tr32(r4[j], r4[j + 2]);
    if ((lane & 8) == 0) {
      const int col = (lane & 7) * 8 + ((lane >> 4) & 1) * 4 + (lane >> 5) * 2;
      *(f32x2*)(sRed2 + w * 64 + col) = f32x2{r2[0], r2[1]};
    }
    LDSWAIT();
    u32 arr;
    CLAIM(arr, cntB);
    asm volatile("s_waitcnt lgkmcnt(0)" : "+v"(arr));
    arr = __builtin_amdgcn_readfirstlane(arr);
    CFENCE();
    if (arr == NWV - 1) {
      float t = 0.f;
#pragma unroll
      for (int i = 0; i < 8; ++i) t += sRed2[i * 64 + lane];
      __builtin_amdgcn_raw_buffer_store_b64(granule(tag, t), rWS, (hs * 64 + lane) * 8, sG2, 16);
    }
  }
  SB();
  STEP2();
  STEP2();
  SB();
#pragma unroll
  for (int j = 0; j < 8; ++j) {
    u32x4 a, c2;
#pragma unroll
    for (int e = 0; e < 2; ++e) { a[2 * e] = pk_lo(wor[2 * j][e], wor[2 * j + 1][e]); a[2 * e + 1] = pk_hi(wor[2 * j][e], wor[2 * j + 1][e]);
                                  c2[2 * e] = pk_lo(wor[2 * j][e + 2], wor[2 * j + 1][e + 2]); c2[2 * e + 1] = pk_hi(wor[2 * j][e + 2], wor[2 * j + 1][e + 2]); }
    *(u32x4*)(smem + L_WO + ((w * 8 + j) * 2) * 1024 + lane * 16) = a;
    *(u32x4*)(smem + L_WO + ((w * 8 + j) * 2 + 1) * 1024 + lane * 16) = c2;
  }
  SB();
  const __amdgpu_buffer_rsrc_t rWGU = RSRC(w < 4 ? p.wg : p.wu, ffn ? DM * FF * 2 : 0);
  const __amdgpu_buffer_rsrc_t rWD = RSRC(p.wd, ffn ? DM * FF * 2 : 0);
  u32 wgr[32], wdr[16];
  {
    const int og = ((w & 3) * 128 + (lane >> 4) * 8) * (FF * 2) + fw * 32 + (lane & 15) * 2;
#pragma unroll
    for (int i = 0; i < 32; ++i) wgr[i] = __builtin_amdgcn_raw_buffer_load_b16(rWGU, og, ((i >> 3) * 32 + (i & 7)) * (FF * 2), 0);
    const int od = (lane >> 4) * 4096 + (w * 64 + (lane & 15)) * 2;
#pragma unroll
    for (int i = 0; i < 16; ++i) wdr[i] = __builtin_amdgcn_raw_buffer_load_b16(rWD, od, (fw * 16 + (i & 3)) * 1024 + (i >> 2) * 32, 0);
  }
  const u32 tlo = (u32)(tag << 16) >> 16, thi = (u32)(tag >> 16);
  u32x4 gn[8];
  const int oN = sG2 + (h * 16 + (lane & 3) * 4) * 8;
#define LOADG2() do { _Pragma("unroll") for (int i_ = 0; i_ < 8; ++i_) gn[i_] = BLD(rWS, oN, (i_ >> 1) * 4096 + (i_ & 1) * 16, 16); } while (0)
#define TAGOK(g_) (((g_)[0] >> 16) == tlo && (g_)[1] == thi && ((g_)[2] >> 16) == tlo && (g_)[3] == thi)
  const u32x2 xw = *(const u32x2*)(p.x + b * DM + h * 16 + (lane & 3) * 4);
  SB();
  STEP2();
  STEP2();
  SB();
  u32 wgp[16], wdp[8];
#pragma unroll
  for (int j = 0; j < 16; ++j) wgp[j] = pk_lo(wgr[2 * j], wgr[2 * j + 1]);
#pragma unroll
  for (int j = 0; j < 8; ++j) wdp[j] = pk_lo(wdr[2 * j], wdr[2 * j + 1]);
#pragma unroll
  for (int j = 0; j < 8; ++j) asm volatile("" : "+v"(wgp[2 * j]), "+v"(wgp[2 * j + 1]), "+v"(wdp[j]));
  SB();
  for (int it = 0; it < G2IT; ++it) { STEP2(); }
  SB();
  LOADG2();
  SB();
  STEP2();
  SB();
  for (unsigned spin = 0;; ++spin) {
    bool ok = true;
#pragma unroll
    for (int i = 0; i < 8; ++i) ok = ok && TAGOK(gn[i]);
    if (__all(ok) || spin > SPIN_CAP) break;
    __builtin_amdgcn_s_sleep(8);
    LOADG2();
  }
  u32 sbcb[4], gab[4];
#pragma unroll
  for (int i = 0; i < 2; ++i) {
    gab[2 * i] = pk_lo(gn[i][0], gn[6 + i][0]); gab[2 * i + 1] = pk_lo(gn[i][2], gn[6 + i][2]);
    sbcb[2 * i] = pk_lo(gn[2 + i][0], gn[4 + i][0]); sbcb[2 * i + 1] = pk_lo(gn[2 + i][2], gn[4 + i][2]);
    if (sbcb[2 * i] == SENT) sbcb[2 * i] = 0x7FC07FC0u;
    if (sbcb[2 * i + 1] == SENT) sbcb[2 * i + 1] = 0x7FC07FC0u;
  }
#pragma unroll
  for (int j = 0; j < 4; ++j) asm volatile("" : "+v"(sbcb[j]), "+v"(gab[j]));
  SB();
  while (tA < NTILE || tB < NTILE) { STEP2(); }
  SB();
  u32x2 fl = __builtin_amdgcn_raw_buffer_load_b64(rWS, (lane & 7) * 8, (int)WS_FL, 16);
  SB();
  STAMP(6);
  STAMPW(14, 7); STAMPW(12, 1); STAMPW(13, 2); STAMPW(3, 3);
  STAMPW(15, 4);

  {
    const float M = x32_max(x16_max(m));
    const float sc = __builtin_amdgcn_exp2f((m - M) * LOG2E);
    const float L = x32_sum(x16_sum(l * sc));
    float v[8];
#pragma unroll
    for (int j = 0; j < 4; ++j) { v[2 * j] = acc[j][0] * sc; v[2 * j + 1] = acc[j][1] * sc; }
    float r4[4], r2[2];
#pragma unroll
    for (int j = 0; j < 4; ++j) r4[j] = tr16(v[j], v[j + 4]);
#pragma unroll
    for (int j = 0; j < 2; ++j) r2[j] = tr32(r4[j], r4[j + 2]);
    const int dd = (lane & 15) * 8 + ((lane >> 4) & 1) * 4 + (lane >> 5) * 2;
    *(f32x2*)(sMrg + w * 132 + 4 + dd) = f32x2{r2[0], r2[1]};
    if (lane == 0) { sMrg[w * 132] = M; sMrg[w * 132 + 1] = L; }
  }
  BAR();
  {
    float Mw[8], M = -INFINITY;
#pragma unroll
    for (int i = 0; i < 8; ++i) { Mw[i] = sMrg[i * 132]; M = fmaxf(M, Mw[i]); }
    float L = 0.f, o = 0.f;
    const int d = w * 16 + (lane & 15);
#pragma unroll
    for (int i = 0; i < 8; ++i) { const float e = __builtin_amdgcn_exp2f((Mw[i] - M) * LOG2E); L = fmaf(e, sMrg[i * 132 + 1], L); o = fmaf(e, sMrg[i * 132 + 4 + d], o); }
    const u32 ob = f2bf(o / L);
    const u32 nb = __builtin_amdgcn_mov_dpp((int)ob, 0xB1  , 0xf, 0xf, true);
    const u32 op = ob | (nb << 16);
    float a[8] = {0, 0, 0, 0, 0, 0, 0, 0};
#pragma unroll
    for (int j = 0; j < 8; ++j) {
      const u32 oj = __builtin_amdgcn_readlane(op, 2 * j);
      const u32x4 w0 = *(const u32x4*)(smem + L_WO + ((w * 8 + j) * 2) * 1024 + lane * 16);
      const u32x4 w1 = *(const u32x4*)(smem + L_WO + ((w * 8 + j) * 2 + 1) * 1024 + lane * 16);
#pragma unroll
      for (int e = 0; e < 4; ++e) { a[e] = dot2(w0[e], oj, a[e]); a[4 + e] = dot2(w1[e], oj, a[4 + e]); }
    }
    *(f32x4*)(sRed + w * 512 + lane * 8) = f32x4{a[0], a[1], a[2], a[3]};
    *(f32x4*)(sRed + w * 512 + lane * 8 + 4) = f32x4{a[4], a[5], a[6], a[7]};
  }
  BAR();
  if (tid < 128) {
    f32x4 t = {0, 0, 0, 0};
#pragma unroll
    for (int i = 0; i < 8; ++i) t += *(const f32x4*)(sRed + i * 512 + tid * 4);
    const u32x4 u = {clean(t[0]), clean(t[1]), clean(t[2]), clean(t[3])};
    __builtin_amdgcn_raw_buffer_store_b128(u, rWS, h * 2048 + tid * 16, sPA, 16);
  }
  STAMP(7);
  if (!ffn && w != 0) return;

  for (unsigned spin = 0;; ++spin) {
    if (__all(fl[0] == (u32)tag && fl[1] == (u32)(tag >> 32)) || spin > SPIN_CAP) break;
    __builtin_amdgcn_s_sleep(2);
    fl = __builtin_amdgcn_raw_buffer_load_b64(rWS, (lane & 7) * 8, (int)WS_FL, 16);
  }
  if (g == 0 && tid == 0) __hip_atomic_store((gu64*)(p.ws + WS_EP), tag, RLX_AGENT);

  float x2[4] = {0, 0, 0, 0};
  if (w == 0) {
    u32x4 ga[2];
    const int off = (lane >> 2) * 2048 + h * 64 + (lane & 3) * 16;
#pragma unroll
    for (int i = 0; i < 2; ++i) ga[i] = BLD(rWS, off, sPA + i * 16 * 2048, 16);
    for (unsigned spin = 0;; ++spin) {
      const bool b0 = has_sent(ga[0]), b1 = has_sent(ga[1]);
      if (!__any(b0 || b1) || spin > SPIN_CAP) break;
      __builtin_amdgcn_s_sleep(A1SLP);
      if (b0) ga[0] = BLD(rWS, off, sPA, 16);
      if (b1) ga[1] = BLD(rWS, off, sPA + 16 * 2048, 16);
    }
    const float xs4[4] = {lo16(xw[0]), hi16(xw[0]), lo16(xw[1]), hi16(xw[1])};
#pragma unroll
    for (int e = 0; e < 4; ++e) {
      float t = __uint_as_float(ga[0][e]) + __uint_as_float(ga[1][e]);
      t += dpp<ROR4>(t); t += dpp<ROR8>(t);
      t = x32_sum(x16_sum(t));
      x2[e] = rb(xs4[e] + rb(lo16(gab[e]) * rb(t)));
    }
    if (lane < 4) {
      __builtin_amdgcn_raw_buffer_store_b128(u32x4{clean(x2[0]), sbcb[0], clean(x2[1]), sbcb[1]}, rWS, lane * 32, (int)WS_X2 + b * 4096 + h * 128, 16);
      __builtin_amdgcn_raw_buffer_store_b128(u32x4{clean(x2[2]), sbcb[2], clean(x2[3]), sbcb[3]}, rWS, lane * 32 + 16, (int)WS_X2 + b * 4096 + h * 128, 16);
    }
  }
  STAMP(8);
  if (!ffn) STAMPFLUSH();
  if (ffn) {
    BAR();
    __builtin_amdgcn_s_sleep(XSLP);
    u32x4 gx[4];
#pragma unroll
    for (int i = 0; i < 4; ++i) gx[i] = BLD(rWS, lane * 16, (int)WS_X2 + w * 4096 + i * 1024, 16);
    for (unsigned spin = 0;; ++spin) {
      bool bad = false;
#pragma unroll
      for (int i = 0; i < 4; ++i) {
        const bool bi = has_sent(gx[i]);
        bad = bad || bi;
        if (bi) gx[i] = BLD(rWS, lane * 16, (int)WS_X2 + w * 4096 + i * 1024, 16);
      }
      if (!__any(bad) || spin > SPIN_CAP) break;
    }
    float xs[8];
#pragma unroll
    for (int j = 0; j < 8; ++j) xs[j] = __uint_as_float(gx[j >> 1][(j & 1) * 2]);
    float meanb, den;
    ln_stats(xs, meanb, den);
    const float rden = 1.0f / den;
    u32 hb[8];
#pragma unroll
    for (int j = 0; j < 8; ++j) { const u32 sc = gx[j >> 1][(j & 1) * 2 + 1]; hb[j] = f2bf(rb(rb(rb(xs[j] - meanb) * rden) * rb(1.0f + hi16(sc))) + lo16(sc)); }
    u32* const sH2 = (u32*)(smem + L_WO);
#pragma unroll
    for (int i = 0; i < 4; ++i) sH2[w * 260 + i * 64 + lane] = hb[2 * i] | (hb[2 * i + 1] << 16);
    BAR();
    STAMP(9);
    f32x4 d = {0, 0, 0, 0};
#pragma unroll
    for (int sx = 0; sx < 4; ++sx) {
      const u32x4 av = *(const u32x4*)(sH2 + (lane & 7) * 260 + (w & 3) * 64 + sx * 16 + (lane >> 4) * 4);
      const u32x4 bv = {wgp[4 * sx], wgp[4 * sx + 1], wgp[4 * sx + 2], wgp[4 * sx + 3]};
      d = __builtin_amdgcn_mfma_f32_16x16x32_bf16(__builtin_bit_cast(bf16x8, av), __builtin_bit_cast(bf16x8, bv), d, 0, 0, 0);
    }
    if (lane < 32) {
#pragma unroll
      for (int i = 0; i < 4; ++i) sRed[w * 128 + ((lane >> 4) * 4 + i) * 16 + (lane & 15)] = d[i];
    }
    BAR();
    u32x2 bq;
    {
      f32x2 gs = {0, 0}, us = {0, 0};
#pragma unroll
      for (int i = 0; i < 4; ++i) { gs += *(const f32x2*)(sRed + i * 128 + lane * 2); us += *(const f32x2*)(sRed + (4 + i) * 128 + lane * 2); }
      const float g0 = rb(gs[0]), g1 = rb(gs[1]);
      const u32 a0 = f2bf(rb(g0 * __builtin_amdgcn_rcpf(1.0f + __expf(-g0))) * rb(us[0])), a1 = f2bf(rb(g1 * __builtin_amdgcn_rcpf(1.0f + __expf(-g1))) * rb(us[1]));
      u32* const sAct = (u32*)(smem + L_WO + 16384) + w * 64;
      sAct[lane] = a0 | (a1 << 16);
      LDSWAIT();
      bq = *(const u32x2*)(sAct + (lane & 7) * 8 + (lane >> 4) * 2);
    }
    {
      const int ob = (lane & 15) < 8 ? (lane & 15) * 2048 + (w * 64 + (lane >> 4) * 4) * 4 : 0x40000000;
#pragma unroll
      for (int q = 0; q < 4; ++q) {
        const f32x4 z = {0, 0, 0, 0};
        const f32x4 o = __builtin_amdgcn_mfma_f32_16x16x16bf16_1k(__builtin_bit_cast(s16x4, u32x2{wdp[2 * q], wdp[2 * q + 1]}), __builtin_bit_cast(s16x4, bq), z, 0, 0, 0);
        const u32x4 u = {clean(o[0]), clean(o[1]), clean(o[2]), clean(o[3])};
        __builtin_amdgcn_raw_buffer_store_b128(u, rWS, ob, (int)WS_PB + fw * 16384 + q * 64, 16);
      }
    }
    STAMP(10);
    STAMPFLUSH();
    if (w != 0) return;
  }
  {
    u32x4 gb[6];
    if (!ffn) {
      __builtin_amdgcn_s_sleep(BSLP);
      const int ot = (int)WS_PB + (g % NFW) * 16384 + b * 2048 + h * 64;
      u32 t0 = __builtin_amdgcn_raw_buffer_load_b32(rWS, 0, ot, 16);
      __builtin_amdgcn_s_sleep(4);
      u32 t1 = __builtin_amdgcn_raw_buffer_load_b32(rWS, 0, ot, 16);
      __builtin_amdgcn_s_sleep(4);
      u32 t2 = __builtin_amdgcn_raw_buffer_load_b32(rWS, 0, ot, 16);
      for (unsigned spin = 0; spin < SPIN_CAP; ++spin) {
        asm volatile("" : "+v"(t0) :: "memory");
        if (__all(t0 != SENT)) break;
        t0 = __builtin_amdgcn_raw_buffer_load_b32(rWS, 0, ot, 16);
        asm volatile("" : "+v"(t1) :: "memory");
        if (__all(t1 != SENT)) break;
        t1 = __builtin_amdgcn_raw_buffer_load_b32(rWS, 0, ot, 16);
        asm volatile("" : "+v"(t2) :: "memory");
        if (__all(t2 != SENT)) break;
        t2 = __builtin_amdgcn_raw_buffer_load_b32(rWS, 0, ot, 16);
      }
    }
    const int off = (lane >> 2) * 16384 + b * 2048 + h * 64 + (lane & 3) * 16;
    const int off5 = (lane >> 2) < NFW - 80 ? off : 0x40000000;
#pragma unroll
    for (int i = 0; i < 6; ++i) gb[i] = BLD(rWS, i < 5 ? off : off5, (int)WS_PB + i * 16 * 16384, 16);
    for (unsigned spin = 0;; ++spin) {
      bool bad = false;
#pragma unroll
      for (int i = 0; i < 6; ++i) {
        const bool bi = has_sent(gb[i]);
        bad = bad || bi;
        if (bi) gb[i] = BLD(rWS, off, (int)WS_PB + i * 16 * 16384, 16);
      }
      if (!__any(bad) || spin > SPIN_CAP) break;
      __builtin_amdgcn_s_sleep(1);
    }
    u32 ob[4];
#pragma unroll
    for (int e = 0; e < 4; ++e) {
      float t = ((__uint_as_float(gb[0][e]) + __uint_as_float(gb[1][e])) + (__uint_as_float(gb[2][e]) + __uint_as_float(gb[3][e]))) + (__uint_as_float(gb[4][e]) + __uint_as_float(gb[5][e]));
      t += dpp<ROR4>(t); t += dpp<ROR8>(t);
      t = x32_sum(x16_sum(t));
      ob[e] = f2bf(x2[e] + rb(hi16(gab[e]) * rb(t)));
    }
    if (lane < 4) *(u32x2*)(p.out + b * DM + h * 16 + lane * 4) = u32x2{ob[0] | (ob[1] << 16), ob[2] | (ob[3] << 16)};
  }
}

extern "C" void kernel_launch(void* const* d_in, const int* in_sizes, int n_in, void* d_out, int out_size, void* d_ws, size_t ws_size,
                              hipStream_t stream) {
  (void)in_sizes; (void)n_in; (void)out_size; (void)ws_size;
  Params p;
  p.x = (const unsigned short*)d_in[0]; p.kc = (const unsigned short*)d_in[1]; p.vc = (const unsigned short*)d_in[2];
  p.wq = (const unsigned short*)d_in[3]; p.wo = (const unsigned short*)d_in[4];
  p.wg = (const unsigned short*)d_in[6]; p.wu = (const unsigned short*)d_in[7]; p.wd = (const unsigned short*)d_in[8];
  p.c = (const unsigned short*)d_in[9]; p.wmod = (const unsigned short*)d_in[10];
  p.out = (unsigned short*)d_out; p.ws = (unsigned char*)d_ws;
  hipFuncSetAttribute((const void*)fused_block_kernel, hipFuncAttributeMaxDynamicSharedMemorySize, LDS_BYTES);
  hipLaunchKernelGGL(fused_block_kernel, dim3(NB * NH), dim3(NT), LDS_BYTES, stream, p);
}
```

```cpp
#include <hip/hip_runtime.h>
#include <hip/hip_bf16.h>
#include <stdint.h>

typedef unsigned int u32;
typedef unsigned long long u64;
using u32x4 = __attribute__((ext_vector_type(4))) unsigned;
using u32x2 = __attribute__((ext_vector_type(2))) unsigned;
using f32x4 = __attribute__((ext_vector_type(4))) float;
using bf16x8 = __attribute__((ext_vector_type(8))) __bf16;
using s16x4 = __attribute__((ext_vector_type(4))) short;
using f32x2 = __attribute__((ext_vector_type(2))) float;
using bf16x2 = __attribute__((ext_vector_type(2))) __bf16;
typedef __attribute__((address_space(1))) unsigned long long gu64;
typedef __attribute__((address_space(1))) unsigned gu32;
#define RLX_AGENT __ATOMIC_RELAXED, __HIP_MEMORY_SCOPE_AGENT

constexpr int NB = 8, DM = 512, NH = 32, SK = 4096, DH = 128, FF = 1376;
constexpr int NT = 512, NWV = 8;
constexpr int NFW = FF / 16;
constexpr int TL = 4;
constexpr int NTILE = SK / (4 * TL);
constexpr u32 SENT = 0xFFFFFFFFu;
constexpr u64 TAGMASK = 0xFFFFFFFFFFFFull;
constexpr unsigned SPIN_CAP = 1u << 20;

constexpr size_t WS_EP = 0;
constexpr size_t WS_FL = 256;
constexpr size_t WS_G1 = 4096;
constexpr size_t WS_G2 = WS_G1 + 8 * 8192;
constexpr size_t WS_PA = WS_G2 + 8 * 16384;
constexpr size_t WS_X2 = WS_PA + 8 * 32 * 2048;
constexpr size_t WS_PB = WS_X2 + 8 * 4096;
constexpr size_t WS_END = WS_PB + (size_t)NFW * 16384;

constexpr int L_WO = 0;
constexpr int L_RED = 131072;
constexpr int L_SC = L_RED + 16384;
constexpr int L_HP = L_SC + 2048;
constexpr int L_Q = L_HP + 1024;
constexpr int L_MRG = L_Q + 256;
constexpr int L_CNT = L_MRG + 8 * 132 * 4;
constexpr int L_RED2 = L_CNT + 16;
constexpr int LDS_BYTES = L_RED2 + 2048 + 256;

struct Params {
  const unsigned short* x; const unsigned short* kc; const unsigned short* vc; const unsigned short* wq; const unsigned short* wo;
  const unsigned short* wg; const unsigned short* wu; const unsigned short* wd; const unsigned short* c; const unsigned short* wmod;
  unsigned short* out; unsigned char* ws;
};

__device__ __forceinline__ float bf2f(unsigned short v) { return __builtin_bit_cast(float, (u32)v << 16); }
__device__ __forceinline__ unsigned short f2bf(float f) { return __builtin_bit_cast(unsigned short, (__bf16)f); }
__device__ __forceinline__ float rb(float f) { return (float)(__bf16)f; }
__device__ __forceinline__ float lo16(u32 x) { return __builtin_bit_cast(float, x << 16); }
__device__ __forceinline__ float hi16(u32 x) { return __builtin_bit_cast(float, x & 0xffff0000u); }
__device__ __forceinline__ float dot2(u32 a, u32 b, float acc) {
  return __builtin_amdgcn_fdot2_f32_bf16(__builtin_bit_cast(bf16x2, a), __builtin_bit_cast(bf16x2, b), acc, false);
}
template <int CTRL> __device__ __forceinline__ float dpp(float x) {
  return __builtin_bit_cast(float, __builtin_amdgcn_mov_dpp(__builtin_bit_cast(int, x), CTRL, 0xf, 0xf, true));
}
constexpr int ROR1 = 0x121, ROR2 = 0x122, ROR4 = 0x124, ROR8 = 0x128;
__device__ __forceinline__ float row16_sum(float x) { x += dpp<ROR8>(x); x += dpp<ROR4>(x); x += dpp<ROR2>(x); x += dpp<ROR1>(x); return x; }
__device__ __forceinline__ float tr16(float a, float b) {
  auto s = __builtin_amdgcn_permlane16_swap(__builtin_bit_cast(u32, a), __builtin_bit_cast(u32, b), false, false);
  return __uint_as_float(s[0]) + __uint_as_float(s[1]);
}
__device__ __forceinline__ float tr32(float a, float b) {
  auto s = __builtin_amdgcn_permlane32_swap(__builtin_bit_cast(u32, a), __builtin_bit_cast(u32, b), false, false);
  return __uint_as_float(s[0]) + __uint_as_float(s[1]);
}
__device__ __forceinline__ float x16_sum(float x) { return tr16(x, x); }
__device__ __forceinline__ float x32_sum(float x) { return tr32(x, x); }
__device__ __forceinline__ float x16_max(float x) {
  auto s = __builtin_amdgcn_permlane16_swap(__builtin_bit_cast(u32, x), __builtin_bit_cast(u32, x), false, false);
  return fmaxf(__uint_as_float(s[0]), __uint_as_float(s[1]));
}
__device__ __forceinline__ float x32_max(float x) {
  auto s = __builtin_amdgcn_permlane32_swap(__builtin_bit_cast(u32, x), __builtin_bit_cast(u32, x), false, false);
  return fmaxf(__uint_as_float(s[0]), __uint_as_float(s[1]));
}
__device__ __forceinline__ float wave_sum(float x) { return x32_sum(x16_sum(row16_sum(x))); }
__device__ __forceinline__ u32 pk_lo(u32 a, u32 b) { return __builtin_amdgcn_perm(b, a, 0x05040100u); }
__device__ __forceinline__ u32 pk_hi(u32 a, u32 b) { return __builtin_amdgcn_perm(b, a, 0x07060302u); }
__device__ __forceinline__ u32 clean(float v) { u32 u = __builtin_bit_cast(u32, v); return u == SENT ? 0x7FC00000u : u; }

__device__ __forceinline__ u32x2 granule(u64 tag, float v) { const u64 g = (tag << 16) | f2bf(v); return u32x2{(u32)g, (u32)(g >> 32)}; }

#define BAR() do { asm volatile("s_waitcnt lgkmcnt(0)" ::: "memory"); __builtin_amdgcn_s_barrier(); asm volatile("" ::: "memory"); } while (0)
#define LDSWAIT() asm volatile("s_waitcnt lgkmcnt(0)" ::: "memory")
#define CFENCE() asm volatile("" ::: "memory")
#define SB() do { asm volatile("" ::: "memory"); __builtin_amdgcn_sched_barrier(0); } while (0)
#define RSRC(p, n) __builtin_amdgcn_make_buffer_rsrc((void*)(p), 0, (n), 0x00020000)
#define BLD(r, vo, so, aux) __builtin_amdgcn_raw_buffer_load_b128((r), (vo), (so), (aux))


__device__ __forceinline__ void ln_stats(const float* xs, float& meanb, float& den) {
  const float mean = wave_sum(((xs[0] + xs[1]) + (xs[2] + xs[3])) + ((xs[4] + xs[5]) + (xs[6] + xs[7]))) * (1.0f / 512.0f);
  float m2 = 0.f;
#pragma unroll
  for (int j = 0; j < 8; ++j) { const float d = xs[j] - mean; m2 = fmaf(d, d, m2); }
  const float var = rb(wave_sum(m2) * (1.0f / 512.0f));
  den = rb(sqrtf(rb(var + 1e-5f)));
  meanb = rb(mean);
}
__device__ __forceinline__ bool has_sent(u32x4 g) { return g[0] == SENT || g[1] == SENT || g[2] == SENT || g[3] == SENT; }

#define HW h
#define WAUXM 0
#define WAUXQ 0
#define WAUXO 0
#define WAUXF 0
#define G2IT 3
#define A1SLP 2
#define BSLP 24
#define XSLP 12
#define NEXTT(TX, tc) (tn += NWV)
#define TMAP(t) (t)
__global__ void __launch_bounds__(NT, 2) fused_block_kernel(Params p) {
  extern __shared__ __attribute__((aligned(16))) unsigned char smem[];
  const int tid = threadIdx.x, lane = tid & 63;
  const int w = __builtin_amdgcn_readfirstlane(tid >> 6);
  const int b = blockIdx.x >> 5, h = blockIdx.x & 31;

  float* const sRed = (float*)(smem + L_RED);
  float* const sSc = (float*)(smem + L_SC);
  float* const sMrg = (float*)(smem + L_MRG);
  float* const sRed2 = (float*)(smem + L_RED2);

  const __amdgpu_buffer_rsrc_t rWS = RSRC(p.ws, (int)WS_END);
  const int g = blockIdx.x;
  const int fw = (g & 7) * 11 + (g >> 3) / 3;
  const bool ffn = (g >> 3) % 3 == 0 && fw < NFW;
  const int sPA = (int)WS_PA + b * 32 * 2048, sG1 = (int)WS_G1 + b * 8192, sG2 = (int)WS_G2 + b * 16384;
  {
    const u32x4 sv = {SENT, SENT, SENT, SENT};
    if (tid < 128) __builtin_amdgcn_raw_buffer_store_b128(sv, rWS, tid * 16, sPA + h * 2048, 16);
    else if (tid < 136) __builtin_amdgcn_raw_buffer_store_b128(sv, rWS, (tid - 128) * 16, (int)WS_X2 + b * 4096 + h * 128, 16);
    if (ffn) {
      __builtin_amdgcn_raw_buffer_store_b128(sv, rWS, tid * 16, (int)WS_PB + fw * 16384, 16);
      __builtin_amdgcn_raw_buffer_store_b128(sv, rWS, tid * 16, (int)WS_PB + fw * 16384 + 8192, 16);
    }
  }
  CFENCE();
  u64 ep = __hip_atomic_load((gu64*)(p.ws + WS_EP), RLX_AGENT);
  const unsigned short cv = p.c[b * DM + tid];
  const unsigned short xv = p.x[b * DM + tid];
  const u32x4 xall = *(const u32x4*)(p.x + b * DM + lane * 8);
  SB();
  const __amdgpu_buffer_rsrc_t rWM = RSRC(p.wmod, DM * 6 * DM * 2);
  const int hs = (h & 7) * 4 + (h >> 3);
  u32x4 wm1[4];
#pragma unroll
  for (int i = 0; i < 4; ++i) wm1[i] = BLD(rWM, (w * 64 + i * 16 + (lane >> 2)) * 6144 + hs * 64 + (lane & 3) * 16, 0, WAUXM);
  SB();
  const __amdgpu_buffer_rsrc_t rK = RSRC(p.kc + (size_t)blockIdx.x * SK * DH, SK * DH * 2);
  const __amdgpu_buffer_rsrc_t rV = RSRC(p.vc + (size_t)blockIdx.x * SK * DH, SK * DH * 2);
  const int vo = lane * 16;
  u32x4 KA[TL], VA[TL], KB[TL], VB[TL];
#define LOADT(dst, r, t) do { _Pragma("unroll") for (int i_ = 0; i_ < TL; ++i_) dst[i_] = BLD(r, vo + i_ * 1024, (t) * (TL * 1024), 2); } while (0)
  LOADT(KA, rK, TMAP(w));
  SB();
  if (tid == 0) { *(u32*)(smem + L_CNT) = 2 * NWV; *(u32*)(smem + L_CNT + 4) = 0; }
  const float cf = bf2f(cv);
  sSc[tid] = rb(cf / (1.0f + __expf(-cf)));
  float y1;
  {
    float xs[8];
#pragma unroll
    for (int j = 0; j < 4; ++j) { xs[2 * j] = lo16(xall[j]); xs[2 * j + 1] = hi16(xall[j]); }
    float meanb, den;
    ln_stats(xs, meanb, den);
    y1 = rb(rb(bf2f(xv) - meanb) / den);
  }
  LDSWAIT();
  {
    float a[8] = {0, 0, 0, 0, 0, 0, 0, 0};
#pragma unroll
    for (int i = 0; i < 4; ++i) {
      const float s = sSc[w * 64 + i * 16 + (lane >> 2)];
#pragma unroll
      for (int j = 0; j < 4; ++j) { a[2 * j] = fmaf(s, lo16(wm1[i][j]), a[2 * j]); a[2 * j + 1] = fmaf(s, hi16(wm1[i][j]), a[2 * j + 1]); }
    }
#pragma unroll
    for (int j = 0; j < 8; ++j) { a[j] += dpp<ROR4>(a[j]); a[j] += dpp<ROR8>(a[j]); }
    float r4[4], r2[2];
#pragma unroll
    for (int j = 0; j < 4; ++j) r4[j] = tr16(a[j], a[j + 4]);
#pragma unroll
    for (int j = 0; j < 2; ++j) r2[j] = tr32(r4[j], r4[j + 2]);
    if ((lane & 12) == 0) {
      const int col = (lane & 3) * 8 + ((lane >> 4) & 1) * 4 + (lane >> 5) * 2;
      *(f32x2*)(sRed + w * 32 + col) = f32x2{r2[0], r2[1]};
    }
  }
  BAR();
  asm volatile("" : "+v"(ep));
  const u64 tag = (ep + 1) & TAGMASK;
  if (tid < 32) {
    float t = 0.f;
#pragma unroll
    for (int i = 0; i < 8; ++i) t += sRed[i * 32 + tid];
    const int col = hs * 32 + tid;
    __builtin_amdgcn_raw_buffer_store_b64(granule(tag, t), rWS, ((col & 511) * 2 + (col >> 9)) * 8, sG1, 16);
  }
  SB();
  const __amdgpu_buffer_rsrc_t rWQ = RSRC(p.wq, DM * NH * DH * 2);
  u32x4 wqr[16];
  {
    const int vq = (w * 64 + (lane >> 4)) * 8192 + h * 256 + (lane & 15) * 16;
#pragma unroll
    for (int i = 0; i < 16; ++i) wqr[i] = BLD(rWQ, vq, ((((i >> 1) + b) & 7) * 8 + (i & 1) * 4) * 8192, WAUXQ);
  }
  SB();
  SB();
  float hval;
  {
    const u32 tlo = (u32)(tag << 16) >> 16, thi = (u32)(tag >> 16);
    u32x4 g = BLD(rWS, tid * 16, sG1, 16);
    SB();
    __builtin_amdgcn_s_barrier();
    SB();
    LOADT(VA, rV, TMAP(w));
    LOADT(KB, rK, TMAP(NWV + w)); LOADT(VB, rV, TMAP(NWV + w));
    SB();
    bool ok = (g[0] >> 16) == tlo && g[1] == thi && (g[2] >> 16) == tlo && g[3] == thi;
    if (!__all(ok)) {
      for (unsigned spin = 0; spin < SPIN_CAP; ++spin) {
        if (!ok) g = BLD(rWS, tid * 16, sG1, 16);
        ok = (g[0] >> 16) == tlo && g[1] == thi && (g[2] >> 16) == tlo && g[3] == thi;
        if (__all(ok)) break;
        __builtin_amdgcn_s_sleep(1);
      }
    }
    const float sa = lo16(g[0]), ca = lo16(g[2]);
    hval = rb(rb(y1 * rb(1.0f + ca)) + sa);
  }
  {
    const int r = lane;
    ((unsigned short*)(smem + L_HP))[((w * 4 + (r & 3)) * 8 + (((r >> 3) - b) & 7)) * 2 + ((r >> 2) & 1)] = f2bf(hval);
    LDSWAIT();
    const u32x4 h0 = *(const u32x4*)(smem + L_HP + (w * 4 + (lane >> 4)) * 32), h1 = *(const u32x4*)(smem + L_HP + (w * 4 + (lane >> 4)) * 32 + 16);
    float a[8] = {0, 0, 0, 0, 0, 0, 0, 0};
#pragma unroll
    for (int j = 0; j < 8; ++j) {
      const u32 hp = j < 4 ? h0[j & 3] : h1[j & 3];
#pragma unroll
      for (int e = 0; e < 4; ++e) {
        a[2 * e] = dot2(pk_lo(wqr[2 * j][e], wqr[2 * j + 1][e]), hp, a[2 * e]);
        a[2 * e + 1] = dot2(pk_hi(wqr[2 * j][e], wqr[2 * j + 1][e]), hp, a[2 * e + 1]);
      }
    }
    float r4[4], r2[2];
#pragma unroll
    for (int j = 0; j < 4; ++j) r4[j] = tr16(a[j], a[j + 4]);
#pragma unroll
    for (int j = 0; j < 2; ++j) r2[j] = tr32(r4[j], r4[j + 2]);
    const int col = (lane & 15) * 8 + ((lane >> 4) & 1) * 4 + (lane >> 5) * 2;
    *(f32x2*)(sRed + 256 + w * 128 + col) = f32x2{r2[0], r2[1]};
    BAR();
    if (tid < 128) {
      float t = 0.f;
#pragma unroll
      for (int i = 0; i < 8; ++i) t += sRed[256 + i * 128 + tid];
      ((unsigned short*)(smem + L_Q))[tid] = f2bf(t);
    }
    if (tid == 128 && h == 0) __hip_atomic_store((gu64*)(p.ws + WS_FL) + b, tag, RLX_AGENT);
    BAR();
  }
  const u32x4 qv = *(const u32x4*)(smem + L_Q + (lane & 15) * 16);

  float m = -INFINITY, l = 0.f;
  f32x2 acc[4] = {{0, 0}, {0, 0}, {0, 0}, {0, 0}};
  int tn = NWV + w;
  int tA = w, tB = NWV + w;
  const u32 cntA = (u32)(uintptr_t)(__attribute__((address_space(3))) unsigned char*)smem + L_CNT, cntB = cntA + 4;
#define CLAIM(r, addr) do { u64 sv_; asm volatile("s_mov_b64 %1, exec\n\ts_mov_b64 exec, 1\n\tds_add_rtn_u32 %0, %2, %3\n\ts_mov_b64 exec, %1" \
                                                  : "=&v"(r), "=&s"(sv_) : "v"(addr), "v"(1u) : "memory"); } while (0)
  constexpr float SCALE = 0.08838834764831845f, LOG2E = 1.4426950408889634f;
#define CLAIMT(r) do { r = 0; } while (0)
#define WAITT(r) do { } while (0)
#define STEPR(KX, VX, TX, RELOAD) do { \
    u32 tc_ = 0; \
    if (RELOAD) CLAIMT(tc_); \
    SB(); \
    const float bias_ = TX < NTILE ? 0.f : -INFINITY; \
    float s_[TL]; \
    _Pragma("unroll") for (int i_ = 0; i_ < TL; ++i_) { float d_ = dot2(KX[i_][0], qv[0], 0.f); d_ = dot2(KX[i_][1], qv[1], d_); d_ = dot2(KX[i_][2], qv[2], d_); s_[i_] = dot2(KX[i_][3], qv[3], d_); } \
    asm volatile("" : "+v"(s_[0]), "+v"(s_[1]), "+v"(s_[2]), "+v"(s_[3])); \
    if (RELOAD) WAITT(tc_); \
    SB(); \
    if (RELOAD) { TX = NEXTT(TX, tc_); LOADT(KX, rK, TMAP(TX)); } \
    SB(); \
    _Pragma("unroll") for (int i_ = 0; i_ < TL; ++i_) s_[i_] = rb(rb(row16_sum(s_[i_])) * SCALE) + bias_; \
    const float mn_ = fmaxf(fmaxf(fmaxf(s_[0], s_[1]), fmaxf(s_[2], s_[3])), m); \
    const float al_ = __builtin_amdgcn_exp2f((m - mn_) * LOG2E); \
    m = mn_; \
    float p_[TL]; \
    _Pragma("unroll") for (int i_ = 0; i_ < TL; ++i_) p_[i_] = __builtin_amdgcn_exp2f((s_[i_] - mn_) * LOG2E); \
    l = fmaf(l, al_, (p_[0] + p_[1]) + (p_[2] + p_[3])); \
    _Pragma("unroll") for (int j_ = 0; j_ < 4; ++j_) acc[j_] *= al_; \
    _Pragma("unroll") for (int i_ = 0; i_ < TL; ++i_) { const f32x2 pp_ = {p_[i_], p_[i_]}; \
      _Pragma("unroll") for (int j_ = 0; j_ < 4; ++j_) acc[j_] = __builtin_elementwise_fma(f32x2{lo16(VX[i_][j_]), hi16(VX[i_][j_])}, pp_, acc[j_]); } \
    asm volatile("" : "+v"(acc[0]), "+v"(acc[1]), "+v"(acc[2]), "+v"(acc[3]), "+v"(m), "+v"(l)); \
    SB(); \
    if (RELOAD) LOADT(VX, rV, TMAP(TX)); \
    SB(); \
  } while (0)
#define STEP(KX, VX, TX) STEPR(KX, VX, TX, true)
#define STEP2() do { STEP(KA, VA, tA); STEP(KB, VB, tB); } while (0)

  STEP2();
  SB();
  u32x4 wm2[8];
#pragma unroll
  for (int i = 0; i < 8; ++i) wm2[i] = BLD(rWM, (w * 64 + i * 8 + (lane >> 3)) * 6144 + 2048 + hs * 128 + (lane & 7) * 16, 0, WAUXM);
  SB();
  STEP2();
  SB();
  const __amdgpu_buffer_rsrc_t rWO = RSRC(p.wo, NH * DH * DM * 2);
  u32x4 wor[16];
#pragma unroll
  for (int i = 0; i < 16; ++i) wor[i] = BLD(rWO, (HW * 128 + w * 16 + i) * 1024 + lane * 16, 0, WAUXO);
  SB();
  {
    float a[8] = {0, 0, 0, 0, 0, 0, 0, 0};
#pragma unroll
    for (int i = 0; i < 8; ++i) {
      const float s = sSc[w * 64 + i * 8 + (lane >> 3)];
#pragma unroll
      for (int j = 0; j < 4; ++j) { a[2 * j] = fmaf(s, lo16(wm2[i][j]), a[2 * j]); a[2 * j + 1] = fmaf(s, hi16(wm2[i][j]), a[2 * j + 1]); }
    }
#pragma unroll
    for (int j = 0; j < 8; ++j) a[j] += dpp<ROR8>(a[j]);
    float r4[4], r2[2];
#pragma unroll
    for (int j = 0; j < 4; ++j) r4[j] = tr16(a[j], a[j + 4]);
#pragma unroll
    for (int j = 0; j < 2; ++j) r2[j] = tr32(r4[j], r4[j + 2]);
    if ((lane & 8) == 0) {
      const int col = (lane & 7) * 8 + ((lane >> 4) & 1) * 4 + (lane >> 5) * 2;
      *(f32x2*)(sRed2 + w * 64 + col) = f32x2{r2[0], r2[1]};
    }
    LDSWAIT();
    u32 arr;
    CLAIM(arr, cntB);
    asm volatile("s_waitcnt lgkmcnt(0)" : "+v"(arr));
    arr = __builtin_amdgcn_readfirstlane(arr);
    CFENCE();
    if (arr == NWV - 1) {
      float t = 0.f;
#pragma unroll
      for (int i = 0; i < 8; ++i) t += sRed2[i * 64 + lane];
      __builtin_amdgcn_raw_buffer_store_b64(granule(tag, t), rWS, (hs * 64 + lane) * 8, sG2, 16);
    }
  }
  SB();
  STEP2();
  STEP2();
  SB();
#pragma unroll
  for (int j = 0; j < 8; ++j) {
    u32x4 a, c2;
#pragma unroll
    for (int e = 0; e < 2; ++e) { a[2 * e] = pk_lo(wor[2 * j][e], wor[2 * j + 1][e]); a[2 * e + 1] = pk_hi(wor[2 * j][e], wor[2 * j + 1][e]);
                                  c2[2 * e] = pk_lo(wor[2 * j][e + 2], wor[2 * j + 1][e + 2]); c2[2 * e + 1] = pk_hi(wor[2 * j][e + 2], wor[2 * j + 1][e + 2]); }
    *(u32x4*)(smem + L_WO + ((w * 8 + j) * 2) * 1024 + lane * 16) = a;
    *(u32x4*)(smem + L_WO + ((w * 8 + j) * 2 + 1) * 1024 + lane * 16) = c2;
  }
  SB();
  const __amdgpu_buffer_rsrc_t rWGU = RSRC(w < 4 ? p.wg : p.wu, ffn ? DM * FF * 2 : 0);
  const __amdgpu_buffer_rsrc_t rWD = RSRC(p.wd, ffn ? DM * FF * 2 : 0);
  u32 wgr[32], wdr[16];
  {
    const int og = ((w & 3) * 128 + (lane >> 4) * 8) * (FF * 2) + fw * 32 + (lane & 15) * 2;
#pragma unroll
    for (int i = 0; i < 32; ++i) wgr[i] = __builtin_amdgcn_raw_buffer_load_b16(rWGU, og, ((i >> 3) * 32 + (i & 7)) * (FF * 2), WAUXF);
    const int od = (lane >> 4) * 4096 + (w * 64 + (lane & 15)) * 2;
#pragma unroll
    for (int i = 0; i < 16; ++i) wdr[i] = __builtin_amdgcn_raw_buffer_load_b16(rWD, od, (fw * 16 + (i & 3)) * 1024 + (i >> 2) * 32, WAUXF);
  }
  const u32 tlo = (u32)(tag << 16) >> 16, thi = (u32)(tag >> 16);
  u32x4 gn[8];
  const int oN = sG2 + (h * 16 + (lane & 3) * 4) * 8;
#define LOADG2() do { _Pragma("unroll") for (int i_ = 0; i_ < 8; ++i_) gn[i_] = BLD(rWS, oN, (i_ >> 1) * 4096 + (i_ & 1) * 16, 16); } while (0)
#define TAGOK(g_) (((g_)[0] >> 16) == tlo && (g_)[1] == thi && ((g_)[2] >> 16) == tlo && (g_)[3] == thi)
  const u32x2 xw = *(const u32x2*)(p.x + b * DM + h * 16 + (lane & 3) * 4);
  SB();
  STEP2();
  STEP2();
  SB();
  u32 wgp[16], wdp[8];
#pragma unroll
  for (int j = 0; j < 16; ++j) wgp[j] = pk_lo(wgr[2 * j], wgr[2 * j + 1]);
#pragma unroll
  for (int j = 0; j < 8; ++j) wdp[j] = pk_lo(wdr[2 * j], wdr[2 * j + 1]);
#pragma unroll
  for (int j = 0; j < 8; ++j) asm volatile("" : "+v"(wgp[2 * j]), "+v"(wgp[2 * j + 1]), "+v"(wdp[j]));
  SB();
  for (int it = 0; it < G2IT; ++it) { STEP2(); }
  SB();
  LOADG2();
  SB();
  STEP2();
  SB();
  for (unsigned spin = 0;; ++spin) {
    bool ok = true;
#pragma unroll
    for (int i = 0; i < 8; ++i) ok = ok && TAGOK(gn[i]);
    if (__all(ok) || spin > SPIN_CAP) break;
    __builtin_amdgcn_s_sleep(8);
    LOADG2();
  }
  u32 sbcb[4], gab[4];
#pragma unroll
  for (int i = 0; i < 2; ++i) {
    gab[2 * i] = pk_lo(gn[i][0], gn[6 + i][0]); gab[2 * i + 1] = pk_lo(gn[i][2], gn[6 + i][2]);
    sbcb[2 * i] = pk_lo(gn[2 + i][0], gn[4 + i][0]); sbcb[2 * i + 1] = pk_lo(gn[2 + i][2], gn[4 + i][2]);
    if (sbcb[2 * i] == SENT) sbcb[2 * i] = 0x7FC07FC0u;
    if (sbcb[2 * i + 1] == SENT) sbcb[2 * i + 1] = 0x7FC07FC0u;
  }
#pragma unroll
  for (int j = 0; j < 4; ++j) asm volatile("" : "+v"(sbcb[j]), "+v"(gab[j]));
  SB();
  while (tA < NTILE || tB < NTILE) { STEP2(); }
  SB();
  u32x2 fl = __builtin_amdgcn_raw_buffer_load_b64(rWS, (lane & 7) * 8, (int)WS_FL, 16);
  SB();

  {
    const float M = x32_max(x16_max(m));
    const float sc = __builtin_amdgcn_exp2f((m - M) * LOG2E);
    const float L = x32_sum(x16_sum(l * sc));
    float v[8];
#pragma unroll
    for (int j = 0; j < 4; ++j) { v[2 * j] = acc[j][0] * sc; v[2 * j + 1] = acc[j][1] * sc; }
    float r4[4], r2[2];
#pragma unroll
    for (int j = 0; j < 4; ++j) r4[j] = tr16(v[j], v[j + 4]);
#pragma unroll
    for (int j = 0; j < 2; ++j) r2[j] = tr32(r4[j], r4[j + 2]);
    const int dd = (lane & 15) * 8 + ((lane >> 4) & 1) * 4 + (lane >> 5) * 2;
    *(f32x2*)(sMrg + w * 132 + 4 + dd) = f32x2{r2[0], r2[1]};
    if (lane == 0) { sMrg[w * 132] = M; sMrg[w * 132 + 1] = L; }
  }
  BAR();
  {
    float Mw[8], M = -INFINITY;
#pragma unroll
    for (int i = 0; i < 8; ++i) { Mw[i] = sMrg[i * 132]; M = fmaxf(M, Mw[i]); }
    float L = 0.f, o = 0.f;
    const int d = w * 16 + (lane & 15);
#pragma unroll
    for (int i = 0; i < 8; ++i) { const float e = __builtin_amdgcn_exp2f((Mw[i] - M) * LOG2E); L = fmaf(e, sMrg[i * 132 + 1], L); o = fmaf(e, sMrg[i * 132 + 4 + d], o); }
    const u32 ob = f2bf(o / L);
    const u32 nb = __builtin_amdgcn_mov_dpp((int)ob, 0xB1  , 0xf, 0xf, true);
    const u32 op = ob | (nb << 16);
    float a[8] = {0, 0, 0, 0, 0, 0, 0, 0};
#pragma unroll
    for (int j = 0; j < 8; ++j) {
      const u32 oj = __builtin_amdgcn_readlane(op, 2 * j);
      const u32x4 w0 = *(const u32x4*)(smem + L_WO + ((w * 8 + j) * 2) * 1024 + lane * 16);
      const u32x4 w1 = *(const u32x4*)(smem + L_WO + ((w * 8 + j) * 2 + 1) * 1024 + lane * 16);
#pragma unroll
      for (int e = 0; e < 4; ++e) { a[e] = dot2(w0[e], oj, a[e]); a[4 + e] = dot2(w1[e], oj, a[4 + e]); }
    }
    *(f32x4*)(sRed + w * 512 + lane * 8) = f32x4{a[0], a[1], a[2], a[3]};
    *(f32x4*)(sRed + w * 512 + lane * 8 + 4) = f32x4{a[4], a[5], a[6], a[7]};
  }
  BAR();
  if (tid < 128) {
    f32x4 t = {0, 0, 0, 0};
#pragma unroll
    for (int i = 0; i < 8; ++i) t += *(const f32x4*)(sRed + i * 512 + tid * 4);
    const u32x4 u = {clean(t[0]), clean(t[1]), clean(t[2]), clean(t[3])};
    __builtin_amdgcn_raw_buffer_store_b128(u, rWS, h * 2048 + tid * 16, sPA, 16);
  }
  if (!ffn && w != 0) return;

  for (unsigned spin = 0;; ++spin) {
    if (__all(fl[0] == (u32)tag && fl[1] == (u32)(tag >> 32)) || spin > SPIN_CAP) break;
    __builtin_amdgcn_s_sleep(2);
    fl = __builtin_amdgcn_raw_buffer_load_b64(rWS, (lane & 7) * 8, (int)WS_FL, 16);
  }
  if (g == 0 && tid == 0) __hip_atomic_store((gu64*)(p.ws + WS_EP), tag, RLX_AGENT);

  float x2[4] = {0, 0, 0, 0};
  if (w == 0) {
    u32x4 ga[2];
    const int off = (lane >> 2) * 2048 + h * 64 + (lane & 3) * 16;
#pragma unroll
    for (int i = 0; i < 2; ++i) ga[i] = BLD(rWS, off, sPA + i * 16 * 2048, 16);
    for (unsigned spin = 0;; ++spin) {
      const bool b0 = has_sent(ga[0]), b1 = has_sent(ga[1]);
      if (!__any(b0 || b1) || spin > SPIN_CAP) break;
      __builtin_amdgcn_s_sleep(A1SLP);
      if (b0) ga[0] = BLD(rWS, off, sPA, 16);
      if (b1) ga[1] = BLD(rWS, off, sPA + 16 * 2048, 16);
    }
    const float xs4[4] = {lo16(xw[0]), hi16(xw[0]), lo16(xw[1]), hi16(xw[1])};
#pragma unroll
    for (int e = 0; e < 4; ++e) {
      float t = __uint_as_float(ga[0][e]) + __uint_as_float(ga[1][e]);
      t += dpp<ROR4>(t); t += dpp<ROR8>(t);
      t = x32_sum(x16_sum(t));
      x2[e] = rb(xs4[e] + rb(lo16(gab[e]) * rb(t)));
    }
    if (lane < 4) {
      __builtin_amdgcn_raw_buffer_store_b128(u32x4{clean(x2[0]), sbcb[0], clean(x2[1]), sbcb[1]}, rWS, lane * 32, (int)WS_X2 + b * 4096 + h * 128, 16);
      __builtin_amdgcn_raw_buffer_store_b128(u32x4{clean(x2[2]), sbcb[2], clean(x2[3]), sbcb[3]}, rWS, lane * 32 + 16, (int)WS_X2 + b * 4096 + h * 128, 16);
    }
  }
  if (ffn) {
    BAR();
    __builtin_amdgcn_s_sleep(XSLP);
    u32x4 gx[4];
#pragma unroll
    for (int i = 0; i < 4; ++i) gx[i] = BLD(rWS, lane * 16, (int)WS_X2 + w * 4096 + i * 1024, 16);
    for (unsigned spin = 0;; ++spin) {
      bool bad = false;
#pragma unroll
      for (int i = 0; i < 4; ++i) {
        const bool bi = has_sent(gx[i]);
        bad = bad || bi;
        if (bi) gx[i] = BLD(rWS, lane * 16, (int)WS_X2 + w * 4096 + i * 1024, 16);
      }
      if (!__any(bad) || spin > SPIN_CAP) break;
    }
    float xs[8];
#pragma unroll
    for (int j = 0; j < 8; ++j) xs[j] = __uint_as_float(gx[j >> 1][(j & 1) * 2]);
    float meanb, den;
    ln_stats(xs, meanb, den);
    const float rden = 1.0f / den;
    u32 hb[8];
#pragma unroll
    for (int j = 0; j < 8; ++j) { const u32 sc = gx[j >> 1][(j & 1) * 2 + 1]; hb[j] = f2bf(rb(rb(rb(xs[j] - meanb) * rden) * rb(1.0f + hi16(sc))) + lo16(sc)); }
    u32* const sH2 = (u32*)(smem + L_WO);
#pragma unroll
    for (int i = 0; i < 4; ++i) sH2[w * 260 + i * 64 + lane] = hb[2 * i] | (hb[2 * i + 1] << 16);
    BAR();
    f32x4 d = {0, 0, 0, 0};
#pragma unroll
    for (int sx = 0; sx < 4; ++sx) {
      const u32x4 av = *(const u32x4*)(sH2 + (lane & 7) * 260 + (w & 3) * 64 + sx * 16 + (lane >> 4) * 4);
      const u32x4 bv = {wgp[4 * sx], wgp[4 * sx + 1], wgp[4 * sx + 2], wgp[4 * sx + 3]};
      d = __builtin_amdgcn_mfma_f32_16x16x32_bf16(__builtin_bit_cast(bf16x8, av), __builtin_bit_cast(bf16x8, bv), d, 0, 0, 0);
    }
    if (lane < 32) {
#pragma unroll
      for (int i = 0; i < 4; ++i) sRed[w * 128 + ((lane >> 4) * 4 + i) * 16 + (lane & 15)] = d[i];
    }
    BAR();
    u32x2 bq;
    {
      f32x2 gs = {0, 0}, us = {0, 0};
#pragma unroll
      for (int i = 0; i < 4; ++i) { gs += *(const f32x2*)(sRed + i * 128 + lane * 2); us += *(const f32x2*)(sRed + (4 + i) * 128 + lane * 2); }
      const float g0 = rb(gs[0]), g1 = rb(gs[1]);
      const u32 a0 = f2bf(rb(g0 * __builtin_amdgcn_rcpf(1.0f + __expf(-g0))) * rb(us[0])), a1 = f2bf(rb(g1 * __builtin_amdgcn_rcpf(1.0f + __expf(-g1))) * rb(us[1]));
      u32* const sAct = (u32*)(smem + L_WO + 16384) + w * 64;
      sAct[lane] = a0 | (a1 << 16);
      LDSWAIT();
      bq = *(const u32x2*)(sAct + (lane & 7) * 8 + (lane >> 4) * 2);
    }
    {
      const int ob = (lane & 15) < 8 ? (lane & 15) * 2048 + (w * 64 + (lane >> 4) * 4) * 4 : 0x40000000;
#pragma unroll
      for (int q = 0; q < 4; ++q) {
        const f32x4 z = {0, 0, 0, 0};
        const f32x4 o = __builtin_amdgcn_mfma_f32_16x16x16bf16_1k(__builtin_bit_cast(s16x4, u32x2{wdp[2 * q], wdp[2 * q + 1]}), __builtin_bit_cast(s16x4, bq), z, 0, 0, 0);
        const u32x4 u = {clean(o[0]), clean(o[1]), clean(o[2]), clean(o[3])};
        __builtin_amdgcn_raw_buffer_store_b128(u, rWS, ob, (int)WS_PB + fw * 16384 + q * 64, 16);
      }
    }
    if (w != 0) return;
  }
  {
    u32x4 gb[6];
    if (!ffn) {
      __builtin_amdgcn_s_sleep(BSLP);
      const int ot = (int)WS_PB + (g % NFW) * 16384 + b * 2048 + h * 64;
      u32 t0 = __builtin_amdgcn_raw_buffer_load_b32(rWS, 0, ot, 16);
      __builtin_amdgcn_s_sleep(4);
      u32 t1 = __builtin_amdgcn_raw_buffer_load_b32(rWS, 0, ot, 16);
      __builtin_amdgcn_s_sleep(4);
      u32 t2 = __builtin_amdgcn_raw_buffer_load_b32(rWS, 0, ot, 16);
      for (unsigned spin = 0; spin < SPIN_CAP; ++spin) {
        asm volatile("" : "+v"(t0) :: "memory");
        if (__all(t0 != SENT)) break;
        t0 = __builtin_amdgcn_raw_buffer_load_b32(rWS, 0, ot, 16);
        asm volatile("" : "+v"(t1) :: "memory");
        if (__all(t1 != SENT)) break;
        t1 = __builtin_amdgcn_raw_buffer_load_b32(rWS, 0, ot, 16);
        asm volatile("" : "+v"(t2) :: "memory");
        if (__all(t2 != SENT)) break;
        t2 = __builtin_amdgcn_raw_buffer_load_b32(rWS, 0, ot, 16);
      }
    }
    const int off = (lane >> 2) * 16384 + b * 2048 + h * 64 + (lane & 3) * 16;
    const int off5 = (lane >> 2) < NFW - 80 ? off : 0x40000000;
#pragma unroll
    for (int i = 0; i < 6; ++i) gb[i] = BLD(rWS, i < 5 ? off : off5, (int)WS_PB + i * 16 * 16384, 16);
    for (unsigned spin = 0;; ++spin) {
      bool bad = false;
#pragma unroll
      for (int i = 0; i < 6; ++i) {
        const bool bi = has_sent(gb[i]);
        bad = bad || bi;
        if (bi) gb[i] = BLD(rWS, off, (int)WS_PB + i * 16 * 16384, 16);
      }
      if (!__any(bad) || spin > SPIN_CAP) break;
      __builtin_amdgcn_s_sleep(1);
    }
    u32 ob[4];
#pragma unroll
    for (int e = 0; e < 4; ++e) {
      float t = ((__uint_as_float(gb[0][e]) + __uint_as_float(gb[1][e])) + (__uint_as_float(gb[2][e]) + __uint_as_float(gb[3][e]))) + (__uint_as_float(gb[4][e]) + __uint_as_float(gb[5][e]));
      t += dpp<ROR4>(t); t += dpp<ROR8>(t);
      t = x32_sum(x16_sum(t));
      ob[e] = f2bf(x2[e] + rb(hi16(gab[e]) * rb(t)));
    }
    if (lane < 4) *(u32x2*)(p.out + b * DM + h * 16 + lane * 4) = u32x2{ob[0] | (ob[1] << 16), ob[2] | (ob[3] << 16)};
  }
}

extern "C" void kernel_launch(void* const* d_in, const int* in_sizes, int n_in, void* d_out, int out_size, void* d_ws, size_t ws_size,
                              hipStream_t stream) {
  (void)in_sizes; (void)n_in; (void)out_size; (void)ws_size;
  Params p;
  p.x = (const unsigned short*)d_in[0]; p.kc = (const unsigned short*)d_in[1]; p.vc = (const unsigned short*)d_in[2];
  p.wq = (const unsigned short*)d_in[3]; p.wo = (const unsigned short*)d_in[4];
  p.wg = (const unsigned short*)d_in[6]; p.wu = (const unsigned short*)d_in[7]; p.wd = (const unsigned short*)d_in[8];
  p.c = (const unsigned short*)d_in[9]; p.wmod = (const unsigned short*)d_in[10];
  p.out = (unsigned short*)d_out; p.ws = (unsigned char*)d_ws;
  hipFuncSetAttribute((const void*)fused_block_kernel, hipFuncAttributeMaxDynamicSharedMemorySize, LDS_BYTES);
  hipLaunchKernelGGL(fused_block_kernel, dim3(NB * NH), dim3(NT), LDS_BYTES, stream, p);
}
```
